```python
import torch

def _softmax(x, axis=-1):
    return torch.softmax(x, dim=axis)
def _gelu(x):
    return torch.nn.functional.gelu(x, approximate="tanh")
def _silu(x):
    return torch.nn.functional.silu(x)
def _sigmoid(x):
    return torch.sigmoid(x)
def _layernorm(x, axis=-1, eps=1e-5):
    m = x.mean(dim=axis, keepdim=True)
    v = x.var(dim=axis, keepdim=True, unbiased=False)
    return (x - m) / torch.sqrt(v + eps)
def _scatter_add(tgt, idx, src, axis=0):
    return tgt.clone().index_add_(axis, idx, src)
def _segment_sum(src, seg, n):
    out = torch.zeros((n,) + src.shape[1:], dtype=src.dtype)
    return out.index_add_(0, seg, src)
def _pad(x, pw):
    flat = tuple(v for pair in reversed(pw) for v in pair)
    return torch.nn.functional.pad(x, flat)

kernel_name = 'parallel_sum__sliding_block_cached__glu_gated_geglu__decode'

def setup_inputs():

    _w = (lambda a: (lambda t: t.to(torch.bfloat16) if t.is_floating_point() else t)(torch.as_tensor(a)))
    def _A_setup():
        B, Sq, D = 1, 1, 1024
        Hq, Hkv, Sk, Dh = 32, 4, 32768, 128
        assert Hq % Hkv == 0, (Hq, Hkv)
        return {
            "x": _w((lambda *s: torch.randn(*s, dtype=torch.bfloat16) * 0.05)(B, Sq, D)),
            "k_cache": _w((lambda *s: torch.randn(*s, dtype=torch.bfloat16) * 1.6)(B, Hkv, Sk, Dh)),
            "v_cache": _w((lambda *s: torch.randn(*s, dtype=torch.bfloat16) * 0.8)(B, Hkv, Sk, Dh)),
            "wq": _w((lambda *s: torch.randn(*s, dtype=torch.bfloat16) * 1.6)(D, Hq * Dh) * (D ** -0.5)),
            "wo": _w((lambda *s: torch.randn(*s, dtype=torch.bfloat16) * 0.8)(Hq * Dh, D) * ((Hq * Dh) ** -0.5)),
        }
    _A_in = _A_setup()

    def _B_setup():
        B, S, Dm, Df = 1, 1, 1024, 3584
        return {
            "x": _w((lambda *s: torch.randn(*s, dtype=torch.bfloat16) * 0.05)(B, S, Dm)),
            "w_gate": _w((lambda *s: torch.randn(*s, dtype=torch.bfloat16) * 0.8)(Dm, Df) * (Dm ** -0.5)),
            "w_up": _w((lambda *s: torch.randn(*s, dtype=torch.bfloat16) * 0.8)(Dm, Df) * (Dm ** -0.5)),
            "w_down": _w((lambda *s: torch.randn(*s, dtype=torch.bfloat16) * 0.8)(Df, Dm) * (Df ** -0.5)),
        }
    _B_in = _B_setup()

    return {
        "A_x": _A_in["x"],
        "A_k_cache": _A_in["k_cache"],
        "A_v_cache": _A_in["v_cache"],
        "A_wq": _A_in["wq"],
        "A_wo": _A_in["wo"],
        "B_x": _B_in["x"],
        "B_w_gate": _B_in["w_gate"],
        "B_w_up": _B_in["w_up"],
        "B_w_down": _B_in["w_down"],
    }

def reference(A_x, A_k_cache, A_v_cache, A_wq, A_wo, B_x, B_w_gate, B_w_up, B_w_down):
    def _A_ref(x, k_cache, v_cache, wq, wo):
        B, Sq, D = x.shape
        Hq, Dh = 32, 128
        Hkv, Sk = k_cache.shape[1], k_cache.shape[2]
        W = min(4096, max(256, Sk // 4))
        rep = Hq // Hkv
        q = (lambda x, *d: x.permute(*d))((x @ wq).reshape(B, Sq, Hq, Dh), 0, 2, 1, 3)
        k = (lambda x, n, axis: torch.repeat_interleave(x, n, dim=axis))(k_cache[:, :, Sk - W:, :], rep, axis=1)
        v = (lambda x, n, axis: torch.repeat_interleave(x, n, dim=axis))(v_cache[:, :, Sk - W:, :], rep, axis=1)
        scores = torch.einsum("bhqd,bhkd->bhqk", q, k) * (Dh ** -0.5)
        p = _softmax(scores, axis=-1)
        o = torch.einsum("bhqk,bhkd->bhqd", p, v)
        return (lambda x, *d: x.permute(*d))(o, 0, 2, 1, 3).reshape(B, Sq, Hq * Dh) @ wo

    def _B_ref(x, w_gate, w_up, w_down):
        g = torch.einsum("bsd,df->bsf", x, w_gate)
        u = torch.einsum("bsd,df->bsf", x, w_up)
        h = _gelu(g) * u
        return torch.einsum("bsf,fd->bsd", h, w_down)

    x = A_x
    xn = _layernorm(x, axis=-1)
    return x + _A_ref(xn, A_k_cache, A_v_cache, A_wq, A_wo) + _B_ref(xn, B_w_gate, B_w_up, B_w_down)
```

```cpp
#include <hip/hip_runtime.h>
#include <hip/hip_bf16.h>
#include <stdint.h>

typedef unsigned short u16;
typedef unsigned v4u __attribute__((ext_vector_type(4)));

__device__ __forceinline__ float bf2f(unsigned b) { return __builtin_bit_cast(float, b << 16); }
__device__ __forceinline__ float bflo(unsigned w) { return __builtin_bit_cast(float, w << 16); }
__device__ __forceinline__ float bfhi(unsigned w) { return __builtin_bit_cast(float, w & 0xffff0000u); }
__device__ __forceinline__ float rbf(float f) { return (float)(__bf16)f; }
__device__ __forceinline__ u16 f2bf(float f) { __bf16 b = (__bf16)f; return __builtin_bit_cast(u16, b); }

constexpr int D = 1024, HQ = 32, HKV = 4, SK = 32768, DH = 128, W = 4096, DF = 3584;

__device__ void ln_block(const u16* x, float* xn, float* red) {
  int t = threadIdx.x;
  float v[4]; float s = 0.f;
  for (int i = 0; i < 4; ++i) { v[i] = bf2f(x[t * 4 + i]); s += v[i]; }
  for (int o = 32; o >= 1; o >>= 1) s += __shfl_xor(s, o);
  if ((t & 63) == 0) red[t >> 6] = s;
  __syncthreads();
  float mean = (red[0] + red[1] + red[2] + red[3]) * (1.f / D);
  __syncthreads();
  float q = 0.f;
  for (int i = 0; i < 4; ++i) { float d = v[i] - mean; q += d * d; }
  for (int o = 32; o >= 1; o >>= 1) q += __shfl_xor(q, o);
  if ((t & 63) == 0) red[t >> 6] = q;
  __syncthreads();
  float var = (red[0] + red[1] + red[2] + red[3]) * (1.f / D);
  float mb = rbf(mean), vb = rbf(var);
  float den = rbf(sqrtf(rbf(vb + 1e-5f)));
  for (int i = 0; i < 4; ++i) xn[t * 4 + i] = rbf(rbf(v[i] - mb) / den);
  __syncthreads();
}

template <int K>
__device__ void gemv64(const float* xin, const u16* Wt, int N, int col0, float* red  , float* outv  ) {
  int t = threadIdx.x, c = t & 7, r = t >> 3;
  float acc[8] = {0, 0, 0, 0, 0, 0, 0, 0};
  for (int k = r; k < K; k += 32) {
    v4u w = *(const v4u*)(Wt + (size_t)k * N + col0 + c * 8);
    float xv = xin[k];
    for (int j = 0; j < 4; ++j) { acc[2 * j] += xv * bflo(w[j]); acc[2 * j + 1] += xv * bfhi(w[j]); }
  }
  for (int j = 0; j < 8; ++j) red[r * 64 + c * 8 + j] = acc[j];
  __syncthreads();
  if (t < 64) { float s = 0.f; for (int i = 0; i < 32; ++i) s += red[i * 64 + t]; outv[t] = s; }
  __syncthreads();
}

__global__ void __launch_bounds__(256) k1(const u16* x, const u16* wq, const u16* wg, const u16* wu, float* q, float* h) {
  __shared__ float xn[D]; __shared__ float red[32 * 64]; __shared__ float o1[64], o2[64];
  ln_block(x, xn, red);
  int b = blockIdx.x;
  if (b < 64) {
    gemv64<D>(xn, wq, HQ * DH, b * 64, red, o1);
    if (threadIdx.x < 64) q[b * 64 + threadIdx.x] = rbf(o1[threadIdx.x]);
  } else {
    b -= 64;
    gemv64<D>(xn, wg, DF, b * 64, red, o1);
    gemv64<D>(xn, wu, DF, b * 64, red, o2);
    if (threadIdx.x < 64) {
      float g = rbf(o1[threadIdx.x]), u = rbf(o2[threadIdx.x]);
      float ge = 0.5f * g * (1.f + tanhf(0.7978845608028654f * (g + 0.044715f * g * g * g)));
      h[b * 64 + threadIdx.x] = rbf(rbf(ge) * u);
    }
  }
}

__global__ void __launch_bounds__(256) k2(const float* q, const u16* kc, const u16* vc, float* o) {
  __shared__ float sc[W]; __shared__ float red[8]; __shared__ float qs[DH]; __shared__ float part[2][DH];
  int hq = blockIdx.x, g = hq / (HQ / HKV), t = threadIdx.x;
  if (t < DH) qs[t] = q[hq * DH + t];
  __syncthreads();
  const u16* kb = kc + ((size_t)g * SK + (SK - W)) * DH;
  const u16* vb = vc + ((size_t)g * SK + (SK - W)) * DH;
  float mx = -INFINITY;
  for (int j = t; j < W; j += 256) {
    float s = 0.f;
    for (int d = 0; d < DH; d += 8) { v4u w = *(const v4u*)(kb + (size_t)j * DH + d);
      for (int i = 0; i < 4; ++i) { s += qs[d + 2 * i] * bflo(w[i]); s += qs[d + 2 * i + 1] * bfhi(w[i]); } }
    s = rbf(rbf(s) * 0.08838834764831845f);
    sc[j] = s; mx = fmaxf(mx, s);
  }
  for (int o2 = 32; o2 >= 1; o2 >>= 1) mx = fmaxf(mx, __shfl_xor(mx, o2));
  if ((t & 63) == 0) red[t >> 6] = mx;
  __syncthreads();
  mx = fmaxf(fmaxf(red[0], red[1]), fmaxf(red[2], red[3]));
  float sum = 0.f;
  for (int j = t; j < W; j += 256) { float e = __expf(sc[j] - mx); sc[j] = e; sum += e; }
  for (int o2 = 32; o2 >= 1; o2 >>= 1) sum += __shfl_xor(sum, o2);
  if ((t & 63) == 0) red[4 + (t >> 6)] = sum;
  __syncthreads();
  sum = red[4] + red[5] + red[6] + red[7];
  float inv = 1.f / sum;
  int d = t & 127, hh = t >> 7;
  float acc = 0.f;
  for (int j = hh; j < W; j += 2) acc += rbf(sc[j] * inv) * bf2f(vb[(size_t)j * DH + d]);
  part[hh][d] = acc;
  __syncthreads();
  if (t < DH) o[hq * DH + t] = rbf(part[0][t] + part[1][t]);
}

__global__ void __launch_bounds__(256) k3(const u16* x, const float* o, const float* h, const u16* wo, const u16* wd, u16* out) {
  __shared__ float xs[HQ * DH]; __shared__ float red[32 * 64]; __shared__ float o1[64], o2[64];
  int b = blockIdx.x, t = threadIdx.x;
  for (int i = t; i < HQ * DH; i += 256) xs[i] = o[i];
  __syncthreads();
  gemv64<HQ * DH>(xs, wo, D, b * 64, red, o1);
  for (int i = t; i < DF; i += 256) xs[i] = h[i];
  __syncthreads();
  gemv64<DF>(xs, wd, D, b * 64, red, o2);
  if (t < 64) {
    float xv = bf2f(x[b * 64 + t]);
    out[b * 64 + t] = f2bf(rbf(xv + rbf(o1[t])) + rbf(o2[t]));
  }
}

extern "C" void kernel_launch(void* const* d_in, const int* in_sizes, int n_in, void* d_out, int out_size, void* d_ws, size_t ws_size, hipStream_t stream) {
  const u16* x = (const u16*)d_in[0];
  const u16* kc = (const u16*)d_in[1];
  const u16* vc = (const u16*)d_in[2];
  const u16* wq = (const u16*)d_in[3];
  const u16* wo = (const u16*)d_in[4];
  const u16* wg = (const u16*)d_in[6];
  const u16* wu = (const u16*)d_in[7];
  const u16* wd = (const u16*)d_in[8];
  float* q = (float*)d_ws; float* h = q + 4096; float* o = h + 4096;
  k1<<<64 + 56, 256, 0, stream>>>(x, wq, wg, wu, q, h);
  k2<<<32, 256, 0, stream>>>(q, kc, vc, o);
  k3<<<16, 256, 0, stream>>>(x, o, h, wo, wd, (u16*)d_out);
}
```

```cpp
#include <hip/hip_runtime.h>
#include <hip/hip_bf16.h>
#include <stdint.h>

typedef unsigned short u16;
typedef unsigned long long u64;
typedef unsigned v4u __attribute__((ext_vector_type(4)));
typedef unsigned v2u __attribute__((ext_vector_type(2)));
typedef float f4 __attribute__((ext_vector_type(4)));
typedef float f2 __attribute__((ext_vector_type(2)));
typedef __bf16 bf8 __attribute__((ext_vector_type(8)));
typedef __bf16 bf2 __attribute__((ext_vector_type(2)));
typedef short s4 __attribute__((ext_vector_type(4)));
#define LAS __attribute__((address_space(3)))
#define DEV __device__ __forceinline__

#define W_AUX 2
#define WA0 6
#define WA1 5
#define WAK 4
#define WA2 6
#define WB0 2
#define NB0 6
#define WB1 4
#define WB2 5
#define X2_WAIT 7
#define X3_WAIT 4
#define X4_DELAY 4
#define POLL_SLEEP 2
#define RED_ROLE 1

constexpr int D = 1024, HQ = 32, HKV = 4, SK = 32768, DH = 128, WIN = 4096, DF = 3584;
constexpr int NWG = 240, NTHR = 512;
constexpr int AUX_SC1 = 16;
constexpr unsigned TAGM = 0x3FFu;
constexpr int SPIN_CAP = 1 << 16;
constexpr float LOG2E = 1.4426950408889634f;

constexpr int WS_E = 0;
constexpr int WS_X1 = 4096;
constexpr int WS_X2O = WS_X1 + 65536;
constexpr int WS_X2M = WS_X2O + 524288;
constexpr int WS_X3 = WS_X2M + 16384;
constexpr int WS_X4 = WS_X3 + 65536;
constexpr int WS_END = WS_X4 + 262144;

constexpr int L_V = 0;
constexpr int L_K = 32768;
constexpr int L_RED = 65536;
constexpr int L_Q = L_RED + 4096;
constexpr int L_P = L_Q + 8 * 272;
constexpr int L_MX = L_P + 8 * 272;
constexpr int L_LS = L_MX + 256;
constexpr int L_RED3 = L_LS + 256;
constexpr int L_END = L_RED3 + 8192;
constexpr int LDS_BYTES = 84 * 1024;
static_assert(L_END <= LDS_BYTES, "lds");

#define STAMP(i) do {} while (0)
#define STAMPV(i, v) do {} while (0)
#define STAMP_DECL
#define STAMP_OUT() do {} while (0)

DEV float asf(unsigned u) { return __builtin_bit_cast(float, u); }
DEV unsigned asu(float f) { return __builtin_bit_cast(unsigned, f); }
DEV float bflo(unsigned w) { return asf(w << 16); }
DEV float bfhi(unsigned w) { return asf(w & 0xffff0000u); }
DEV unsigned pk2(float lo, float hi) { f2 v = {lo, hi}; return __builtin_bit_cast(unsigned, __builtin_convertvector(v, bf2)); }
DEV float rbf(float f) { return asf(pk2(f, f) & 0xffff0000u); }
template <int C> DEV float dppf(float x) { return asf(__builtin_amdgcn_mov_dpp(asu(x), C, 0xf, 0xf, true)); }
template <int C> DEV unsigned dppu(unsigned x) { return __builtin_amdgcn_mov_dpp(x, C, 0xf, 0xf, true); }
constexpr int XOR1 = 0xB1, XOR2 = 0x4E, HMIR = 0x141, ROR4 = 0x124, ROR8 = 0x128, NBC = 0x150, QB0 = 0x00, QB1 = 0x55;
DEV float swap16_add(float a, float b) { auto s = __builtin_amdgcn_permlane16_swap(asu(a), asu(b), false, false); return asf(s[0]) + asf(s[1]); }
DEV float swap32_add(float a, float b) { auto s = __builtin_amdgcn_permlane32_swap(asu(a), asu(b), false, false); return asf(s[0]) + asf(s[1]); }
DEV float x16_sum(float x) { return swap16_add(x, x); }
DEV float x32_sum(float x) { return swap32_add(x, x); }
DEV float x16_max(float x) { auto s = __builtin_amdgcn_permlane16_swap(asu(x), asu(x), false, false); return fmaxf(asf(s[0]), asf(s[1])); }
DEV float x32_max(float x) { auto s = __builtin_amdgcn_permlane32_swap(asu(x), asu(x), false, false); return fmaxf(asf(s[0]), asf(s[1])); }
DEV float row_sum(float x) { x += dppf<XOR1>(x); x += dppf<XOR2>(x); x += dppf<HMIR>(x); x += dppf<ROR8>(x); return x; }
DEV float wave_sum(float x) { return x32_sum(x16_sum(row_sum(x))); }
DEV f4 mfma(v4u a, v4u b, f4 c) { return __builtin_amdgcn_mfma_f32_16x16x32_bf16(__builtin_bit_cast(bf8, a), __builtin_bit_cast(bf8, b), c, 0, 0, 0); }
#define SB() __builtin_amdgcn_sched_barrier(0)
#define BAR() asm volatile("s_waitcnt lgkmcnt(0)\n\ts_barrier" ::: "memory")
#define BAR0() asm volatile("s_barrier" ::: "memory")
#define CFENCE() asm volatile("" ::: "memory")
#define TOUCH(x) asm volatile("" : "+v"(x))

typedef __amdgpu_buffer_rsrc_t rsrc_t;
DEV rsrc_t mkrs(const void* p, unsigned bytes) { return __builtin_amdgcn_make_buffer_rsrc((void*)p, 0, bytes, 0x00020000); }
DEV v4u ldw(rsrc_t r, int voff, int soff) { return __builtin_amdgcn_raw_buffer_load_b128(r, voff, soff, W_AUX); }
DEV v4u ldx(rsrc_t r, int voff) { return __builtin_amdgcn_raw_buffer_load_b128(r, voff, 0, AUX_SC1); }
DEV void stx(rsrc_t r, int voff, v4u v) { __builtin_amdgcn_raw_buffer_store_b128(v, r, voff, 0, AUX_SC1); }
DEV unsigned tag1(float f, unsigned T) { return ((asu(f) + 0x200u) & ~TAGM) | T; }
DEV v4u tag4(f4 v, unsigned T) { return v4u{tag1(v[0], T), tag1(v[1], T), tag1(v[2], T), tag1(v[3], T)}; }
DEV unsigned bad4(v4u v, unsigned T) { return ((v[0] ^ T) | (v[1] ^ T) | (v[2] ^ T) | (v[3] ^ T)) & TAGM; }
DEV f4 val4(v4u v) { return f4{asf(v[0] & ~TAGM), asf(v[1] & ~TAGM), asf(v[2] & ~TAGM), asf(v[3] & ~TAGM)}; }

template <int I> struct IC { static constexpr int v = I; };
template <int LO, int HI, class F> DEV void sfor(F&& f) { if constexpr (LO < HI) { f(IC<LO>{}); sfor<LO + 1, HI>(f); } }
constexpr int imin(int a, int b) { return a < b ? a : b; }
constexpr int imax(int a, int b) { return a > b ? a : b; }
constexpr int upA(int t) { return t < 0 ? WA0 : imin(17 + WA2, imax(upA(t - 1), t + 1 + (t < 7 ? WA1 : WAK))); }
constexpr int upB(int t) { return t < 0 ? WB0 : imin(16 + WB2, imax(upB(t - 1), t + 1 + (t + 1 < NB0 ? WB0 : WB1))); }

struct Masks { unsigned m[4]; };
DEV v4u aop(unsigned xr, const Masks& k) { return v4u{xr & k.m[0], xr & k.m[1], xr & k.m[2], xr & k.m[3]}; }

DEV unsigned ln_own(v4u xa, v4u xb, unsigned own) {
  float v[16];
#pragma unroll
  for (int i = 0; i < 4; ++i) { v[2 * i] = bflo(xa[i]); v[2 * i + 1] = bfhi(xa[i]); v[8 + 2 * i] = bflo(xb[i]); v[9 + 2 * i] = bfhi(xb[i]); }
  float s = 0.f;
#pragma unroll
  for (int i = 0; i < 16; ++i) s += v[i];
  const float mean = wave_sum(s) * (1.f / D);
  float q = 0.f;
#pragma unroll
  for (int i = 0; i < 16; ++i) { const float d = v[i] - mean; q = fmaf(d, d, q); }
  const float var = wave_sum(q) * (1.f / D);
  const float den = rbf(__builtin_sqrtf(rbf(rbf(var) + 1e-5f)));
  const float xn = rbf(asf(own << 16) - rbf(mean)) / den;
  return pk2(xn, xn);
}

DEV unsigned voff_b(unsigned row, unsigned ch) { return 256u * row + 16u * (ch ^ (((row & 3) << 2) | ((row >> 2) & 3))); }

__global__ void __launch_bounds__(NTHR, 2)
fused_k(const u16* x, const u16* kc, const u16* vc, const u16* wq, const u16* wo, const u16* wg, const u16* wu, const u16* wd,
        u16* out, unsigned char* ws
) {
  __shared__ __attribute__((aligned(16))) unsigned char lds_raw[LDS_BYTES];
  LAS unsigned char* L = (LAS unsigned char*)lds_raw;
  const int tid = threadIdx.x, lane = tid & 63, w = __builtin_amdgcn_readfirstlane(tid >> 6);
  const int gp = lane >> 4, c = lane & 15;
  const int kg = 2 * (gp & 1) + (gp >> 1);
  const int b = blockIdx.x, xcd = b & 7, li = b >> 3;
  STAMP_DECL
  STAMP(0);
  const rsrc_t rW = mkrs(ws, WS_END);
  const rsrc_t rX = mkrs(x, D * 2);
  v4u xa = __builtin_amdgcn_raw_buffer_load_b128(rX, lane * 16, 0, 0);
  v4u xb = __builtin_amdgcn_raw_buffer_load_b128(rX, lane * 16 + 1024, 0, 0);
  const unsigned e0 = __builtin_amdgcn_raw_buffer_load_b32(rW, WS_E, 0, 0);
  unsigned T;
  Masks mk;
  auto setup = [&]() {
    T = (__builtin_amdgcn_readfirstlane(e0) + 1u) & TAGM;
    int cc = c; TOUCH(cc);
#pragma unroll
    for (int d = 0; d < 4; ++d) mk.m[d] = (cc == 2 * d) ? 0x0000ffffu : (cc == 2 * d + 1) ? 0xffff0000u : 0u;
  };
  unsigned xl;
  v4u wl[8];
  int slot, cq;

  if (li < 16) {
    const int g = xcd >> 1, j = (xcd & 1) * 16 + li;
    const int ct = j & 7, ks = j >> 3;
    const unsigned own = __builtin_amdgcn_raw_buffer_load_b16(rX, (256 * ks + 32 * w + 8 * gp + (c & 7)) * 2, 0, 0);
    cq = j & 3; slot = 8 * g + (j >> 2);
    v4u wqv[8], kv[4], vv[4], q0, q1;
    const rsrc_t rq = mkrs(wq, D * HQ * DH * 2), rk = mkrs(kc, HKV * SK * DH * 2), rv = mkrs(vc, HKV * SK * DH * 2), ro = mkrs(wo, HQ * DH * D * 2);
    const int voq = (256 * ks + 32 * w + 8 * gp) * 8192 + ((8 * g + ct) * 128 + 8 * c) * 2;
    int vokv, voo, x1off;
    auto issue = [&](auto I) {
      constexpr int k = decltype(I)::v;
      if constexpr (k < 8) wqv[k] = ldw(rq, voq, k * 8192);
      else if constexpr (k < 12) kv[k - 8] = ldw(rk, vokv + (k - 8) * 1024, 0);
      else if constexpr (k < 16) vv[k - 12] = ldw(rv, vokv + (k - 12) * 1024, 0);
      else if constexpr (k == 16) { q0 = ldx(rW, x1off); q1 = ldx(rW, x1off + 4096); }
      else wl[k - 17] = ldw(ro, voo + ((k - 17) & 1) * 256, ((k - 17) >> 1) * 8192);
    };
    sfor<0, WA0>([&](auto I) { SB(); BAR0(); issue(I); });
    SB();
    vokv = ((g * SK + (SK - WIN) + 128 * j + 16 * w) * DH) * 2 + lane * 16;
    voo = (128 * slot + 16 * w + kg) * 2048 + (256 * cq + 8 * c) * 2;
    x1off = WS_X1 + ((g * 4 + 2 * (lane >> 5)) * 1024 + w * 128) * 4 + (lane & 31) * 16;
    TOUCH(xa); TOUCH(xb);
    const unsigned xn = ln_own(xa, xb, own);
    setup();
    SB();
    STAMP(1);
    f4 acc = {0.f, 0.f, 0.f, 0.f};
    sfor<0, 8>([&](auto I) {
      constexpr int t = decltype(I)::v;
      acc = mfma(aop(dppu<NBC + t>(xn), mk), wqv[t], acc);
      SB();
      sfor<upA(t - 1), upA(t)>(issue);
      SB();
    });
    if (lane < 32) *(LAS f4*)(L + L_RED + w * 512 + (8 * c + 4 * gp) * 4) = acc;
    STAMP(2);
    BAR();
    if (w == 0) {
      const int hf = lane >> 5, ch = lane & 31;
      f4 s = *(LAS f4*)(L + L_RED + (4 * hf) * 512 + ch * 16);
#pragma unroll
      for (int i = 1; i < 4; ++i) s += *(LAS f4*)(L + L_RED + (4 * hf + i) * 512 + ch * 16);
      s[0] = x32_sum(s[0]); s[1] = x32_sum(s[1]); s[2] = x32_sum(s[2]); s[3] = x32_sum(s[3]);
      if (lane < 32) stx(rW, WS_X1 + ((g * 4 + ks) * 1024 + ct * 128) * 4 + ch * 16, tag4(s, T));
    }
    SB();
    STAMP(3);
    sfor<8, 16>([&](auto I) {
      constexpr int t = decltype(I)::v;
      if constexpr (t < 12) *(LAS v4u*)(L + L_K + voff_b(16 * w + 4 * (t - 8) + gp, c)) = kv[t - 8];
      else *(LAS v4u*)(L + L_V + voff_b(16 * w + 4 * (t - 12) + gp, c)) = vv[t - 12];
      SB();
      sfor<upA(t - 1), upA(t)>(issue);
      if constexpr (t == 11) {
#pragma unroll
        for (int i = 0; i < 4; ++i) kv[i] = *(LAS v4u*)(L + L_K + voff_b(16 * w + c, 4 * i + gp));
      }
      SB();
    });
    sfor<upA(15), 17 + WA2>(issue);
    SB();
    STAMP(4);
    {
      int spins = 0;
      unsigned bad = bad4(q0, T) | bad4(q1, T);
      if (__builtin_expect(__any(bad != 0), 0)) {
        do {
          CFENCE();
          if (bad != 0) { q0 = ldx(rW, x1off); q1 = ldx(rW, x1off + 4096); bad = bad4(q0, T) | bad4(q1, T); }
        } while (__any(bad != 0) & (++spins < SPIN_CAP));
        ++spins;
      }
      STAMPV(20, spins);
    }
    STAMP(5);
    {
      const f4 s = val4(q0) + val4(q1);
      const float a = swap32_add(s[0], s[2]), bq = swap32_add(s[1], s[3]);
      *(LAS unsigned*)(L + L_Q + w * 272 + (lane & 31) * 8 + (lane >> 5) * 4) = pk2(a, bq);
    }
    BAR();
    STAMP(12);
    f4 sc, sc2;
    {
      const LAS unsigned char* qp = L + L_Q + (c & 7) * 272 + gp * 16;
      const v4u qa = *(LAS v4u*)(qp), qb = *(LAS v4u*)(qp + 64), qc = *(LAS v4u*)(qp + 128), qd = *(LAS v4u*)(qp + 192);
      sc = mfma(kv[0], qa, f4{0.f, 0.f, 0.f, 0.f});
      sc2 = mfma(kv[1], qb, f4{0.f, 0.f, 0.f, 0.f});
      sc = mfma(kv[2], qc, sc);
      sc2 = mfma(kv[3], qd, sc2);
      sc += sc2;
    }
    {
      const unsigned a = pk2(sc[0], sc[1]), bq = pk2(sc[2], sc[3]);
      const unsigned a2 = pk2(bflo(a) * 0.08838834764831845f, bfhi(a) * 0.08838834764831845f);
      const unsigned b2 = pk2(bflo(bq) * 0.08838834764831845f, bfhi(bq) * 0.08838834764831845f);
      sc = f4{bflo(a2), bfhi(a2), bflo(b2), bfhi(b2)};
    }
    float mw = fmaxf(fmaxf(sc[0], sc[1]), fmaxf(sc[2], sc[3]));
    mw = x32_max(x16_max(mw));
    if (lane < 8) *(LAS float*)(L + L_MX + (lane * 8 + w) * 4) = mw;
    STAMP(13);
    BAR();
    float m;
    {
      const f4 a = *(LAS f4*)(L + L_MX + (c & 7) * 32), bq = *(LAS f4*)(L + L_MX + (c & 7) * 32 + 16);
      m = fmaxf(fmaxf(fmaxf(a[0], a[1]), fmaxf(a[2], a[3])), fmaxf(fmaxf(bq[0], bq[1]), fmaxf(bq[2], bq[3])));
    }
    float p[4];
#pragma unroll
    for (int r = 0; r < 4; ++r) p[r] = __builtin_amdgcn_exp2f((sc[r] - m) * LOG2E);
    if (c < 8) *(LAS v2u*)(L + L_P + c * 272 + (16 * w + 4 * gp) * 2) = v2u{pk2(p[0], p[1]), pk2(p[2], p[3])};
    float lw = (p[0] + p[1]) + (p[2] + p[3]);
    lw = x32_sum(x16_sum(lw));
    if (lane < 8) *(LAS float*)(L + L_LS + (lane * 8 + w) * 4) = lw;
    STAMP(14);
    BAR();
    f4 ov, ov2;
    {
      v4u va[4], pa[4];
#pragma unroll
      for (int t = 0; t < 4; ++t) {
        const int row = 32 * t + 8 * gp + (c >> 2), ch = 2 * w + ((c & 3) >> 1);
        const s4 a0 = __builtin_amdgcn_ds_read_tr16_b64_v4i16((LAS s4*)(L + L_V + voff_b(row, ch) + 8 * (c & 1)));
        const s4 a1 = __builtin_amdgcn_ds_read_tr16_b64_v4i16((LAS s4*)(L + L_V + voff_b(row + 4, ch) + 8 * (c & 1)));
        const v2u u0 = __builtin_bit_cast(v2u, a0), u1 = __builtin_bit_cast(v2u, a1);
        va[t] = v4u{u0[0], u0[1], u1[0], u1[1]};
        pa[t] = *(LAS v4u*)(L + L_P + (c & 7) * 272 + t * 64 + gp * 16);
      }
      ov = mfma(va[0], pa[0], f4{0.f, 0.f, 0.f, 0.f});
      ov2 = mfma(va[1], pa[1], f4{0.f, 0.f, 0.f, 0.f});
      ov = mfma(va[2], pa[2], ov);
      ov2 = mfma(va[3], pa[3], ov2);
      ov += ov2;
    }
    if (c < 8) stx(rW, WS_X2O + ((((g * 8 + c) * 8 + w) * 32 + j) * 16 + 4 * gp) * 4, tag4(ov, T));
    if (w == 0 && lane < 8) {
      const f4 a = *(LAS f4*)(L + L_LS + lane * 32), bq = *(LAS f4*)(L + L_LS + lane * 32 + 16);
      const float l = ((a[0] + a[1]) + (a[2] + a[3])) + ((bq[0] + bq[1]) + (bq[2] + bq[3]));
      stx(rW, WS_X2M + ((g * 8 + lane) * 32 + j) * 16, tag4(f4{m, l, 0.f, 0.f}, T));
    }
    STAMP(6);
    sfor<17 + WA2, 25>(issue);
    SB();
    if constexpr (X2_WAIT > 0) TOUCH(wl[X2_WAIT - 1]);
    SB();
    STAMP(17);
    {
      const int oo = WS_X2O + (slot * 8 + w) * 2048 + lane * 16;
      const int mo = WS_X2M + (slot * 32 + (lane >> 2) + 16 * (lane & 1)) * 16;
      v4u ml = ldx(rW, mo), o0 = ldx(rW, oo), o1 = ldx(rW, oo + 1024);
      unsigned bad = bad4(o0, T) | bad4(o1, T) | bad4(ml, T);
      int spins = 0;
      if (__any(bad != 0)) {
        do {
          CFENCE();
          __builtin_amdgcn_s_sleep(POLL_SLEEP);
          if (bad != 0) { ml = ldx(rW, mo); o0 = ldx(rW, oo); o1 = ldx(rW, oo + 1024); bad = bad4(o0, T) | bad4(o1, T) | bad4(ml, T); }
        } while (__any(bad != 0) & (++spins < SPIN_CAP));
        ++spins;
      }
      STAMPV(21, spins);
      STAMP(7);
      const float ms = asf(ml[0] & ~TAGM), ls = asf(ml[1] & ~TAGM);
      float M = fmaxf(ms, dppf<XOR1>(ms));
      M = fmaxf(M, dppf<ROR4>(M)); M = fmaxf(M, dppf<ROR8>(M));
      M = x32_max(x16_max(M));
      const float e = __builtin_amdgcn_exp2f((ms - M) * LOG2E);
      float Lt = e * ls;
      Lt += dppf<XOR1>(Lt); Lt += dppf<ROR4>(Lt); Lt += dppf<ROR8>(Lt);
      Lt = x32_sum(x16_sum(Lt));
      const f4 z = val4(o0) * dppf<QB0>(e) + val4(o1) * dppf<QB1>(e);
      float o = swap16_add(swap32_add(z[0], z[1]), swap32_add(z[2], z[3]));
      o += dppf<ROR4>(o); o += dppf<ROR8>(o);
      o = o / Lt;
      xl = pk2(o, o);
    }
  } else {
    const int idx = xcd * 14 + (li - 16), G = idx >> 2, j = idx & 3;
    const int kh = j & 1;
    const unsigned own = __builtin_amdgcn_raw_buffer_load_b16(rX, (512 * kh + 64 * w + lane) * 2, 0, 0);
    cq = j; slot = 32 + G;
    v4u gu[16];
    const rsrc_t rg = mkrs((j >> 1) ? wu : wg, D * DF * 2), rd = mkrs(wd, DF * D * 2);
    const int vog = (512 * kh + 64 * w + 16 * gp) * 7168 + (128 * G + 8 * c) * 2;
    int vod;
    auto issue = [&](auto I) {
      constexpr int k = decltype(I)::v;
      if constexpr (k < 16) gu[k] = ldw(rg, vog, k * 7168);
      else wl[k - 16] = ldw(rd, vod + ((k - 16) & 1) * 256, ((k - 16) >> 1) * 8192);
    };
    sfor<0, WB0>([&](auto I) { SB(); BAR0(); issue(I); });
    SB();
    vod = (128 * G + 16 * w + kg) * 2048 + (256 * cq + 8 * c) * 2;
    TOUCH(xa); TOUCH(xb);
    const unsigned xn = ln_own(xa, xb, own);
    setup();
    SB();
    STAMP(1);
    f4 acc = {0.f, 0.f, 0.f, 0.f};
    sfor<0, 16>([&](auto I) {
      constexpr int t = decltype(I)::v;
      acc = mfma(aop(dppu<NBC + t>(xn), mk), gu[t], acc);
      SB();
      sfor<upB(t - 1), upB(t)>(issue);
      SB();
    });
    sfor<upB(15), 16 + WB2>(issue);
    SB();
    if (lane < 32) *(LAS f4*)(L + L_RED + w * 512 + (8 * c + 4 * gp) * 4) = acc;
    STAMP(2);
    BAR();
    if (w == 0) {
      const int hf = lane >> 5, ch = lane & 31;
      f4 s = *(LAS f4*)(L + L_RED + (4 * hf) * 512 + ch * 16);
#pragma unroll
      for (int i = 1; i < 4; ++i) s += *(LAS f4*)(L + L_RED + (4 * hf + i) * 512 + ch * 16);
      s[0] = x32_sum(s[0]); s[1] = x32_sum(s[1]); s[2] = x32_sum(s[2]); s[3] = x32_sum(s[3]);
      if (lane < 32) stx(rW, WS_X3 + (G * 4 + j) * 512 + ch * 16, tag4(s, T));
    }
    SB();
    STAMP(3);
    sfor<16 + WB2, 24>([&](auto I) { constexpr int k = decltype(I)::v; TOUCH(wl[k - 16 - WB2]); SB(); issue(I); SB(); });
    if constexpr (X3_WAIT > 0) TOUCH(wl[X3_WAIT - 1]);
    SB();
    STAMP(4);
    const int ho = WS_X3 + (G * 4 + ((c >> 1) & 2) + (c >> 3)) * 512 + (4 * w + (c & 3)) * 16;
    v4u hv;
    int spins = 0;
    do {
      CFENCE();
      hv = ldx(rW, ho);
    } while (__any(bad4(hv, T) != 0) & (++spins < SPIN_CAP));
    STAMPV(21, spins);
    STAMP(7);
    {
      const unsigned e01 = (gp & 2) ? hv[1] : hv[0], e23 = (gp & 2) ? hv[3] : hv[2];
      const float pv = asf(((gp & 1) ? e23 : e01) & ~TAGM);
      const float s = pv + dppf<ROR8>(pv);
      const float o = dppf<ROR4>(s);
      const float gg = rbf((c & 4) ? o : s), uu = rbf((c & 4) ? s : o);
      const float y = gg * fmaf(gg * gg, 0.044715f * 0.7978845608028654f * -2.f * LOG2E, 0.7978845608028654f * -2.f * LOG2E);
      const float ge = gg * __builtin_amdgcn_rcpf(1.f + __builtin_amdgcn_exp2f(y));
      const float hh = rbf(ge) * uu;
      xl = pk2(hh, hh);
    }
  }

  {
    f4 a0 = {0.f, 0.f, 0.f, 0.f}, a1 = {0.f, 0.f, 0.f, 0.f};
    sfor<0, 4>([&](auto I) {
      constexpr int t = decltype(I)::v;
      const v4u a = aop(dppu<NBC + t>(xl), mk);
      a0 = mfma(a, wl[2 * t], a0);
      a1 = mfma(a, wl[2 * t + 1], a1);
    });
    if (lane < 32) {
      *(LAS f4*)(L + L_RED3 + w * 1024 + (8 * c + 4 * gp) * 4) = a0;
      *(LAS f4*)(L + L_RED3 + w * 1024 + 512 + (8 * c + 4 * gp) * 4) = a1;
    }
    STAMP(8);
    BAR();
    if (w == 0) {
      f4 s = *(LAS f4*)(L + L_RED3 + lane * 16);
#pragma unroll
      for (int i = 1; i < 8; ++i) s += *(LAS f4*)(L + L_RED3 + i * 1024 + lane * 16);
      stx(rW, WS_X4 + (cq * 64 + slot) * 1024 + lane * 16, tag4(s, T));
      STAMP(9);
    }
  }
  const int rid = RED_ROLE == 0 ? (li < 8 ? li * 8 + xcd : 64) : (li >= 16 && li < 24 ? (li - 16) * 8 + xcd : 64);
  if (w == 1 && rid < 64) {
    const int off = WS_X4 + ((rid >> 4) * 64 + (lane >> 2)) * 1024 + (rid & 15) * 64 + (lane & 3) * 16;
    const int off3 = lane < 48 ? off + 48 * 1024 : off;
    const int col = rid * 16 + 4 * (c & 3) + ((c >> 2) & 2) + (gp & 1);
    const unsigned xo = __builtin_amdgcn_raw_buffer_load_b16(rX, col * 2, 0, 0);
    if (X4_DELAY > 0) __builtin_amdgcn_s_sleep(X4_DELAY);
    v4u v0 = ldx(rW, off), v1 = ldx(rW, off + 16 * 1024), v2 = ldx(rW, off + 32 * 1024), v3 = ldx(rW, off3);
    unsigned b0 = bad4(v0, T), b1 = bad4(v1, T), b2 = bad4(v2, T), b3 = bad4(v3, T);
    int spins = 0;
    while (__any((b0 | b1 | b2 | b3) != 0) & (++spins < SPIN_CAP)) {
      CFENCE();
      if (b0 != 0) v0 = ldx(rW, off);
      if (b1 != 0) v1 = ldx(rW, off + 16 * 1024);
      if (b2 != 0) v2 = ldx(rW, off + 32 * 1024);
      if (b3 != 0) v3 = ldx(rW, off3);
      b0 = bad4(v0, T); b1 = bad4(v1, T); b2 = bad4(v2, T); b3 = bad4(v3, T);
    }
    STAMPV(22, spins);
    STAMP(10);
    const f4 ya = val4(v0) + val4(v1);
    f4 yb = val4(v3);
    if (lane >= 48) yb = f4{0.f, 0.f, 0.f, 0.f};
    yb += val4(v2);
    const float z0 = swap32_add(ya[0], yb[0]), z1 = swap32_add(ya[1], yb[1]), z2 = swap32_add(ya[2], yb[2]), z3 = swap32_add(ya[3], yb[3]);
    float y0 = swap16_add(z0, z1), y1 = swap16_add(z2, z3);
    y0 += dppf<ROR4>(y0); y1 += dppf<ROR4>(y1);
    y0 += dppf<ROR8>(y0); y1 += dppf<ROR8>(y1);
    const float z = (c & 8) ? y1 : y0;
    auto sw = __builtin_amdgcn_permlane32_swap(asu(z), asu(z), false, false);
    const float res = rbf(asf(xo << 16) + rbf(asf(sw[0]))) + rbf(asf(sw[1]));
    if (lane < 32 && (c & 4) == 0) out[col] = (u16)(pk2(res, res) & 0xffffu);
    if (rid == 0 && lane == 0) __builtin_amdgcn_raw_buffer_store_b32(T, rW, WS_E, 0, AUX_SC1);
    STAMP(11);
  }
  STAMP_OUT();
}

extern "C" void kernel_launch(void* const* d_in, const int* in_sizes, int n_in, void* d_out, int out_size, void* d_ws, size_t ws_size,
                              hipStream_t stream) {
  fused_k<<<NWG, NTHR, 0, stream>>>((const u16*)d_in[0], (const u16*)d_in[1], (const u16*)d_in[2], (const u16*)d_in[3], (const u16*)d_in[4],
                                    (const u16*)d_in[6], (const u16*)d_in[7], (const u16*)d_in[8], (u16*)d_out, (unsigned char*)d_ws);
}
```

```cpp
#define AMP 1
#define RED_ROLE 1
#include <hip/hip_runtime.h>
#include <hip/hip_bf16.h>
#include <stdint.h>

typedef unsigned short u16;
typedef unsigned long long u64;
typedef unsigned v4u __attribute__((ext_vector_type(4)));
typedef unsigned v2u __attribute__((ext_vector_type(2)));
typedef float f4 __attribute__((ext_vector_type(4)));
typedef float f2 __attribute__((ext_vector_type(2)));
typedef __bf16 bf8 __attribute__((ext_vector_type(8)));
typedef __bf16 bf2 __attribute__((ext_vector_type(2)));
typedef short s4 __attribute__((ext_vector_type(4)));
#define LAS __attribute__((address_space(3)))
#define DEV __device__ __forceinline__

#define W_AUX 2
#define WA0 6
#define WA1 5
#define WAK 4
#define WA2 6
#define WB0 2
#define NB0 6
#define WB1 4
#define WB2 5
#define X2_WAIT 7
#define X3_WAIT 4
#define X4_DELAY 4
#define POLL_SLEEP 2

constexpr int D = 1024, HQ = 32, HKV = 4, SK = 32768, DH = 128, WIN = 4096, DF = 3584;
constexpr int NWG = 240, NTHR = 512;
constexpr int AUX_SC1 = 16;
constexpr unsigned TAGM = 0x3FFu;
constexpr int SPIN_CAP = 1 << 16;
constexpr float LOG2E = 1.4426950408889634f;

constexpr int WS_E = 0;
constexpr int WS_X1 = 4096;
constexpr int WS_X2O = WS_X1 + 65536;
constexpr int WS_X2M = WS_X2O + 524288;
constexpr int WS_X3 = WS_X2M + 16384;
constexpr int WS_X4 = WS_X3 + 65536;
constexpr int WS_END = WS_X4 + 262144;

constexpr int L_V = 0;
constexpr int L_K = 32768;
constexpr int L_RED = 65536;
constexpr int L_Q = L_RED + 4096;
constexpr int L_P = L_Q + 8 * 272;
constexpr int L_MX = L_P + 8 * 272;
constexpr int L_LS = L_MX + 256;
constexpr int L_RED3 = L_LS + 256;
constexpr int L_END = L_RED3 + 8192;
constexpr int LDS_BYTES = 84 * 1024;
static_assert(L_END <= LDS_BYTES, "lds");

#define STAMP(i) do { if ((i) == 0 || (i) == AMP) asm volatile("s_memrealtime %0\n\ts_waitcnt lgkmcnt(0)" : "=s"(st_[(i) == 0 ? 0 : 1]) :: "memory"); } while (0)
#define STAMPV(i, v) do {} while (0)
#define STAMP_DECL u64 st_[2] = {0,0};
#define STAMP_OUT() do { if (w == 1 && rid == 0) { u64 now_; const u64 until_ = st_[0] + 20 * (st_[1] - st_[0]); do { __builtin_amdgcn_s_sleep(1); asm volatile("s_memrealtime %0\n\ts_waitcnt lgkmcnt(0)" : "=s"(now_) :: "memory"); } while (now_ < until_); } } while (0)

DEV float asf(unsigned u) { return __builtin_bit_cast(float, u); }
DEV unsigned asu(float f) { return __builtin_bit_cast(unsigned, f); }
DEV float bflo(unsigned w) { return asf(w << 16); }
DEV float bfhi(unsigned w) { return asf(w & 0xffff0000u); }
DEV unsigned pk2(float lo, float hi) { f2 v = {lo, hi}; return __builtin_bit_cast(unsigned, __builtin_convertvector(v, bf2)); }
DEV float rbf(float f) { return asf(pk2(f, f) & 0xffff0000u); }
template <int C> DEV float dppf(float x) { return asf(__builtin_amdgcn_mov_dpp(asu(x), C, 0xf, 0xf, true)); }
template <int C> DEV unsigned dppu(unsigned x) { return __builtin_amdgcn_mov_dpp(x, C, 0xf, 0xf, true); }
constexpr int XOR1 = 0xB1, XOR2 = 0x4E, HMIR = 0x141, ROR4 = 0x124, ROR8 = 0x128, NBC = 0x150, QB0 = 0x00, QB1 = 0x55;
DEV float swap16_add(float a, float b) { auto s = __builtin_amdgcn_permlane16_swap(asu(a), asu(b), false, false); return asf(s[0]) + asf(s[1]); }
DEV float swap32_add(float a, float b) { auto s = __builtin_amdgcn_permlane32_swap(asu(a), asu(b), false, false); return asf(s[0]) + asf(s[1]); }
DEV float x16_sum(float x) { return swap16_add(x, x); }
DEV float x32_sum(float x) { return swap32_add(x, x); }
DEV float x16_max(float x) { auto s = __builtin_amdgcn_permlane16_swap(asu(x), asu(x), false, false); return fmaxf(asf(s[0]), asf(s[1])); }
DEV float x32_max(float x) { auto s = __builtin_amdgcn_permlane32_swap(asu(x), asu(x), false, false); return fmaxf(asf(s[0]), asf(s[1])); }
DEV float row_sum(float x) { x += dppf<XOR1>(x); x += dppf<XOR2>(x); x += dppf<HMIR>(x); x += dppf<ROR8>(x); return x; }
DEV float wave_sum(float x) { return x32_sum(x16_sum(row_sum(x))); }
DEV f4 mfma(v4u a, v4u b, f4 c) { return __builtin_amdgcn_mfma_f32_16x16x32_bf16(__builtin_bit_cast(bf8, a), __builtin_bit_cast(bf8, b), c, 0, 0, 0); }
#define SB() __builtin_amdgcn_sched_barrier(0)
#define BAR() asm volatile("s_waitcnt lgkmcnt(0)\n\ts_barrier" ::: "memory")
#define BAR0() asm volatile("s_barrier" ::: "memory")
#define CFENCE() asm volatile("" ::: "memory")
#define TOUCH(x) asm volatile("" : "+v"(x))

typedef __amdgpu_buffer_rsrc_t rsrc_t;
DEV rsrc_t mkrs(const void* p, unsigned bytes) { return __builtin_amdgcn_make_buffer_rsrc((void*)p, 0, bytes, 0x00020000); }
DEV v4u ldw(rsrc_t r, int voff, int soff) { return __builtin_amdgcn_raw_buffer_load_b128(r, voff, soff, W_AUX); }
DEV v4u ldx(rsrc_t r, int voff) { return __builtin_amdgcn_raw_buffer_load_b128(r, voff, 0, AUX_SC1); }
DEV void stx(rsrc_t r, int voff, v4u v) { __builtin_amdgcn_raw_buffer_store_b128(v, r, voff, 0, AUX_SC1); }
DEV unsigned tag1(float f, unsigned T) { return ((asu(f) + 0x200u) & ~TAGM) | T; }
DEV v4u tag4(f4 v, unsigned T) { return v4u{tag1(v[0], T), tag1(v[1], T), tag1(v[2], T), tag1(v[3], T)}; }
DEV unsigned bad4(v4u v, unsigned T) { return ((v[0] ^ T) | (v[1] ^ T) | (v[2] ^ T) | (v[3] ^ T)) & TAGM; }
DEV f4 val4(v4u v) { return f4{asf(v[0] & ~TAGM), asf(v[1] & ~TAGM), asf(v[2] & ~TAGM), asf(v[3] & ~TAGM)}; }

template <int I> struct IC { static constexpr int v = I; };
template <int LO, int HI, class F> DEV void sfor(F&& f) { if constexpr (LO < HI) { f(IC<LO>{}); sfor<LO + 1, HI>(f); } }
constexpr int imin(int a, int b) { return a < b ? a : b; }
constexpr int imax(int a, int b) { return a > b ? a : b; }
constexpr int upA(int t) { return t < 0 ? WA0 : imin(17 + WA2, imax(upA(t - 1), t + 1 + (t < 7 ? WA1 : WAK))); }
constexpr int upB(int t) { return t < 0 ? WB0 : imin(16 + WB2, imax(upB(t - 1), t + 1 + (t + 1 < NB0 ? WB0 : WB1))); }

struct Masks { unsigned m[4]; };
DEV v4u aop(unsigned xr, const Masks& k) { return v4u{xr & k.m[0], xr & k.m[1], xr & k.m[2], xr & k.m[3]}; }

DEV unsigned ln_own(v4u xa, v4u xb, unsigned own) {
  float v[16];
#pragma unroll
  for (int i = 0; i < 4; ++i) { v[2 * i] = bflo(xa[i]); v[2 * i + 1] = bfhi(xa[i]); v[8 + 2 * i] = bflo(xb[i]); v[9 + 2 * i] = bfhi(xb[i]); }
  float s = 0.f;
#pragma unroll
  for (int i = 0; i < 16; ++i) s += v[i];
  const float mean = wave_sum(s) * (1.f / D);
  float q = 0.f;
#pragma unroll
  for (int i = 0; i < 16; ++i) { const float d = v[i] - mean; q = fmaf(d, d, q); }
  const float var = wave_sum(q) * (1.f / D);
  const float den = rbf(__builtin_sqrtf(rbf(rbf(var) + 1e-5f)));
  const float xn = rbf(asf(own << 16) - rbf(mean)) / den;
  return pk2(xn, xn);
}

DEV unsigned voff_b(unsigned row, unsigned ch) { return 256u * row + 16u * (ch ^ (((row & 3) << 2) | ((row >> 2) & 3))); }

__global__ void __launch_bounds__(NTHR, 2)
fused_k(const u16* x, const u16* kc, const u16* vc, const u16* wq, const u16* wo, const u16* wg, const u16* wu, const u16* wd,
        u16* out, unsigned char* ws
) {
  __shared__ __attribute__((aligned(16))) unsigned char lds_raw[LDS_BYTES];
  LAS unsigned char* L = (LAS unsigned char*)lds_raw;
  const int tid = threadIdx.x, lane = tid & 63, w = __builtin_amdgcn_readfirstlane(tid >> 6);
  const int gp = lane >> 4, c = lane & 15;
  const int kg = 2 * (gp & 1) + (gp >> 1);
  const int b = blockIdx.x, xcd = b & 7, li = b >> 3;
  STAMP_DECL
  STAMP(0);
  const rsrc_t rW = mkrs(ws, WS_END);
  const rsrc_t rX = mkrs(x, D * 2);
  v4u xa = __builtin_amdgcn_raw_buffer_load_b128(rX, lane * 16, 0, 0);
  v4u xb = __builtin_amdgcn_raw_buffer_load_b128(rX, lane * 16 + 1024, 0, 0);
  const unsigned e0 = __builtin_amdgcn_raw_buffer_load_b32(rW, WS_E, 0, 0);
  unsigned T;
  Masks mk;
  auto setup = [&]() {
    T = (__builtin_amdgcn_readfirstlane(e0) + 1u) & TAGM;
    int cc = c; TOUCH(cc);
#pragma unroll
    for (int d = 0; d < 4; ++d) mk.m[d] = (cc == 2 * d) ? 0x0000ffffu : (cc == 2 * d + 1) ? 0xffff0000u : 0u;
  };
  unsigned xl;
  v4u wl[8];
  int slot, cq;

  if (li < 16) {
    const int g = xcd >> 1, j = (xcd & 1) * 16 + li;
    const int ct = j & 7, ks = j >> 3;
    const unsigned own = __builtin_amdgcn_raw_buffer_load_b16(rX, (256 * ks + 32 * w + 8 * gp + (c & 7)) * 2, 0, 0);
    cq = j & 3; slot = 8 * g + (j >> 2);
    v4u wqv[8], kv[4], vv[4], q0, q1;
    const rsrc_t rq = mkrs(wq, D * HQ * DH * 2), rk = mkrs(kc, HKV * SK * DH * 2), rv = mkrs(vc, HKV * SK * DH * 2), ro = mkrs(wo, HQ * DH * D * 2);
    const int voq = (256 * ks + 32 * w + 8 * gp) * 8192 + ((8 * g + ct) * 128 + 8 * c) * 2;
    int vokv, voo, x1off;
    auto issue = [&](auto I) {
      constexpr int k = decltype(I)::v;
      if constexpr (k < 8) wqv[k] = ldw(rq, voq, k * 8192);
      else if constexpr (k < 12) kv[k - 8] = ldw(rk, vokv + (k - 8) * 1024, 0);
      else if constexpr (k < 16) vv[k - 12] = ldw(rv, vokv + (k - 12) * 1024, 0);
      else if constexpr (k == 16) { q0 = ldx(rW, x1off); q1 = ldx(rW, x1off + 4096); }
      else wl[k - 17] = ldw(ro, voo + ((k - 17) & 1) * 256, ((k - 17) >> 1) * 8192);
    };
    sfor<0, WA0>([&](auto I) { SB(); BAR0(); issue(I); });
    SB();
    vokv = ((g * SK + (SK - WIN) + 128 * j + 16 * w) * DH) * 2 + lane * 16;
    voo = (128 * slot + 16 * w + kg) * 2048 + (256 * cq + 8 * c) * 2;
    x1off = WS_X1 + ((g * 4 + 2 * (lane >> 5)) * 1024 + w * 128) * 4 + (lane & 31) * 16;
    TOUCH(xa); TOUCH(xb);
    const unsigned xn = ln_own(xa, xb, own);
    setup();
    SB();
    STAMP(1);
    f4 acc = {0.f, 0.f, 0.f, 0.f};
    sfor<0, 8>([&](auto I) {
      constexpr int t = decltype(I)::v;
      acc = mfma(aop(dppu<NBC + t>(xn), mk), wqv[t], acc);
      SB();
      sfor<upA(t - 1), upA(t)>(issue);
      SB();
    });
    if (lane < 32) *(LAS f4*)(L + L_RED + w * 512 + (8 * c + 4 * gp) * 4) = acc;
    STAMP(2);
    BAR();
    if (w == 0) {
      const int hf = lane >> 5, ch = lane & 31;
      f4 s = *(LAS f4*)(L + L_RED + (4 * hf) * 512 + ch * 16);
#pragma unroll
      for (int i = 1; i < 4; ++i) s += *(LAS f4*)(L + L_RED + (4 * hf + i) * 512 + ch * 16);
      s[0] = x32_sum(s[0]); s[1] = x32_sum(s[1]); s[2] = x32_sum(s[2]); s[3] = x32_sum(s[3]);
      if (lane < 32) stx(rW, WS_X1 + ((g * 4 + ks) * 1024 + ct * 128) * 4 + ch * 16, tag4(s, T));
    }
    SB();
    STAMP(3);
    sfor<8, 16>([&](auto I) {
      constexpr int t = decltype(I)::v;
      if constexpr (t < 12) *(LAS v4u*)(L + L_K + voff_b(16 * w + 4 * (t - 8) + gp, c)) = kv[t - 8];
      else *(LAS v4u*)(L + L_V + voff_b(16 * w + 4 * (t - 12) + gp, c)) = vv[t - 12];
      SB();
      sfor<upA(t - 1), upA(t)>(issue);
      if constexpr (t == 11) {
#pragma unroll
        for (int i = 0; i < 4; ++i) kv[i] = *(LAS v4u*)(L + L_K + voff_b(16 * w + c, 4 * i + gp));
      }
      SB();
    });
    sfor<upA(15), 17 + WA2>(issue);
    SB();
    STAMP(4);
    {
      int spins = 0;
      unsigned bad = bad4(q0, T) | bad4(q1, T);
      if (__builtin_expect(__any(bad != 0), 0)) {
        do {
          CFENCE();
          if (bad != 0) { q0 = ldx(rW, x1off); q1 = ldx(rW, x1off + 4096); bad = bad4(q0, T) | bad4(q1, T); }
        } while (__any(bad != 0) & (++spins < SPIN_CAP));
        ++spins;
      }
      STAMPV(20, spins);
    }
    STAMP(5);
    {
      const f4 s = val4(q0) + val4(q1);
      const float a = swap32_add(s[0], s[2]), bq = swap32_add(s[1], s[3]);
      *(LAS unsigned*)(L + L_Q + w * 272 + (lane & 31) * 8 + (lane >> 5) * 4) = pk2(a, bq);
    }
    BAR();
    STAMP(12);
    f4 sc, sc2;
    {
      const LAS unsigned char* qp = L + L_Q + (c & 7) * 272 + gp * 16;
      const v4u qa = *(LAS v4u*)(qp), qb = *(LAS v4u*)(qp + 64), qc = *(LAS v4u*)(qp + 128), qd = *(LAS v4u*)(qp + 192);
      sc = mfma(kv[0], qa, f4{0.f, 0.f, 0.f, 0.f});
      sc2 = mfma(kv[1], qb, f4{0.f, 0.f, 0.f, 0.f});
      sc = mfma(kv[2], qc, sc);
      sc2 = mfma(kv[3], qd, sc2);
      sc += sc2;
    }
    {
      const unsigned a = pk2(sc[0], sc[1]), bq = pk2(sc[2], sc[3]);
      const unsigned a2 = pk2(bflo(a) * 0.08838834764831845f, bfhi(a) * 0.08838834764831845f);
      const unsigned b2 = pk2(bflo(bq) * 0.08838834764831845f, bfhi(bq) * 0.08838834764831845f);
      sc = f4{bflo(a2), bfhi(a2), bflo(b2), bfhi(b2)};
    }
    float mw = fmaxf(fmaxf(sc[0], sc[1]), fmaxf(sc[2], sc[3]));
    mw = x32_max(x16_max(mw));
    if (lane < 8) *(LAS float*)(L + L_MX + (lane * 8 + w) * 4) = mw;
    STAMP(13);
    BAR();
    float m;
    {
      const f4 a = *(LAS f4*)(L + L_MX + (c & 7) * 32), bq = *(LAS f4*)(L + L_MX + (c & 7) * 32 + 16);
      m = fmaxf(fmaxf(fmaxf(a[0], a[1]), fmaxf(a[2], a[3])), fmaxf(fmaxf(bq[0], bq[1]), fmaxf(bq[2], bq[3])));
    }
    float p[4];
#pragma unroll
    for (int r = 0; r < 4; ++r) p[r] = __builtin_amdgcn_exp2f((sc[r] - m) * LOG2E);
    if (c < 8) *(LAS v2u*)(L + L_P + c * 272 + (16 * w + 4 * gp) * 2) = v2u{pk2(p[0], p[1]), pk2(p[2], p[3])};
    float lw = (p[0] + p[1]) + (p[2] + p[3]);
    lw = x32_sum(x16_sum(lw));
    if (lane < 8) *(LAS float*)(L + L_LS + (lane * 8 + w) * 4) = lw;
    STAMP(14);
    BAR();
    f4 ov, ov2;
    {
      v4u va[4], pa[4];
#pragma unroll
      for (int t = 0; t < 4; ++t) {
        const int row = 32 * t + 8 * gp + (c >> 2), ch = 2 * w + ((c & 3) >> 1);
        const s4 a0 = __builtin_amdgcn_ds_read_tr16_b64_v4i16((LAS s4*)(L + L_V + voff_b(row, ch) + 8 * (c & 1)));
        const s4 a1 = __builtin_amdgcn_ds_read_tr16_b64_v4i16((LAS s4*)(L + L_V + voff_b(row + 4, ch) + 8 * (c & 1)));
        const v2u u0 = __builtin_bit_cast(v2u, a0), u1 = __builtin_bit_cast(v2u, a1);
        va[t] = v4u{u0[0], u0[1], u1[0], u1[1]};
        pa[t] = *(LAS v4u*)(L + L_P + (c & 7) * 272 + t * 64 + gp * 16);
      }
      ov = mfma(va[0], pa[0], f4{0.f, 0.f, 0.f, 0.f});
      ov2 = mfma(va[1], pa[1], f4{0.f, 0.f, 0.f, 0.f});
      ov = mfma(va[2], pa[2], ov);
      ov2 = mfma(va[3], pa[3], ov2);
      ov += ov2;
    }
    if (c < 8) stx(rW, WS_X2O + ((((g * 8 + c) * 8 + w) * 32 + j) * 16 + 4 * gp) * 4, tag4(ov, T));
    if (w == 0 && lane < 8) {
      const f4 a = *(LAS f4*)(L + L_LS + lane * 32), bq = *(LAS f4*)(L + L_LS + lane * 32 + 16);
      const float l = ((a[0] + a[1]) + (a[2] + a[3])) + ((bq[0] + bq[1]) + (bq[2] + bq[3]));
      stx(rW, WS_X2M + ((g * 8 + lane) * 32 + j) * 16, tag4(f4{m, l, 0.f, 0.f}, T));
    }
    STAMP(6);
    sfor<17 + WA2, 25>(issue);
    SB();
    if constexpr (X2_WAIT > 0) TOUCH(wl[X2_WAIT - 1]);
    SB();
    STAMP(17);
    {
      const int oo = WS_X2O + (slot * 8 + w) * 2048 + lane * 16;
      const int mo = WS_X2M + (slot * 32 + (lane >> 2) + 16 * (lane & 1)) * 16;
      v4u ml = ldx(rW, mo), o0 = ldx(rW, oo), o1 = ldx(rW, oo + 1024);
      unsigned bad = bad4(o0, T) | bad4(o1, T) | bad4(ml, T);
      int spins = 0;
      if (__any(bad != 0)) {
        do {
          CFENCE();
          __builtin_amdgcn_s_sleep(POLL_SLEEP);
          if (bad != 0) { ml = ldx(rW, mo); o0 = ldx(rW, oo); o1 = ldx(rW, oo + 1024); bad = bad4(o0, T) | bad4(o1, T) | bad4(ml, T); }
        } while (__any(bad != 0) & (++spins < SPIN_CAP));
        ++spins;
      }
      STAMPV(21, spins);
      STAMP(7);
      const float ms = asf(ml[0] & ~TAGM), ls = asf(ml[1] & ~TAGM);
      float M = fmaxf(ms, dppf<XOR1>(ms));
      M = fmaxf(M, dppf<ROR4>(M)); M = fmaxf(M, dppf<ROR8>(M));
      M = x32_max(x16_max(M));
      const float e = __builtin_amdgcn_exp2f((ms - M) * LOG2E);
      float Lt = e * ls;
      Lt += dppf<XOR1>(Lt); Lt += dppf<ROR4>(Lt); Lt += dppf<ROR8>(Lt);
      Lt = x32_sum(x16_sum(Lt));
      const f4 z = val4(o0) * dppf<QB0>(e) + val4(o1) * dppf<QB1>(e);
      float o = swap16_add(swap32_add(z[0], z[1]), swap32_add(z[2], z[3]));
      o += dppf<ROR4>(o); o += dppf<ROR8>(o);
      o = o / Lt;
      xl = pk2(o, o);
    }
  } else {
    const int idx = xcd * 14 + (li - 16), G = idx >> 2, j = idx & 3;
    const int kh = j & 1;
    const unsigned own = __builtin_amdgcn_raw_buffer_load_b16(rX, (512 * kh + 64 * w + lane) * 2, 0, 0);
    cq = j; slot = 32 + G;
    v4u gu[16];
    const rsrc_t rg = mkrs((j >> 1) ? wu : wg, D * DF * 2), rd = mkrs(wd, DF * D * 2);
    const int vog = (512 * kh + 64 * w + 16 * gp) * 7168 + (128 * G + 8 * c) * 2;
    int vod;
    auto issue = [&](auto I) {
      constexpr int k = decltype(I)::v;
      if constexpr (k < 16) gu[k] = ldw(rg, vog, k * 7168);
      else wl[k - 16] = ldw(rd, vod + ((k - 16) & 1) * 256, ((k - 16) >> 1) * 8192);
    };
    sfor<0, WB0>([&](auto I) { SB(); BAR0(); issue(I); });
    SB();
    vod = (128 * G + 16 * w + kg) * 2048 + (256 * cq + 8 * c) * 2;
    TOUCH(xa); TOUCH(xb);
    const unsigned xn = ln_own(xa, xb, own);
    setup();
    SB();
    STAMP(1);
    f4 acc = {0.f, 0.f, 0.f, 0.f};
    sfor<0, 16>([&](auto I) {
      constexpr int t = decltype(I)::v;
      acc = mfma(aop(dppu<NBC + t>(xn), mk), gu[t], acc);
      SB();
      sfor<upB(t - 1), upB(t)>(issue);
      SB();
    });
    sfor<upB(15), 16 + WB2>(issue);
    SB();
    if (lane < 32) *(LAS f4*)(L + L_RED + w * 512 + (8 * c + 4 * gp) * 4) = acc;
    STAMP(2);
    BAR();
    if (w == 0) {
      const int hf = lane >> 5, ch = lane & 31;
      f4 s = *(LAS f4*)(L + L_RED + (4 * hf) * 512 + ch * 16);
#pragma unroll
      for (int i = 1; i < 4; ++i) s += *(LAS f4*)(L + L_RED + (4 * hf + i) * 512 + ch * 16);
      s[0] = x32_sum(s[0]); s[1] = x32_sum(s[1]); s[2] = x32_sum(s[2]); s[3] = x32_sum(s[3]);
      if (lane < 32) stx(rW, WS_X3 + (G * 4 + j) * 512 + ch * 16, tag4(s, T));
    }
    SB();
    STAMP(3);
    sfor<16 + WB2, 24>([&](auto I) { constexpr int k = decltype(I)::v; TOUCH(wl[k - 16 - WB2]); SB(); issue(I); SB(); });
    if constexpr (X3_WAIT > 0) TOUCH(wl[X3_WAIT - 1]);
    SB();
    STAMP(4);
    const int ho = WS_X3 + (G * 4 + ((c >> 1) & 2) + (c >> 3)) * 512 + (4 * w + (c & 3)) * 16;
    v4u hv;
    int spins = 0;
    do {
      CFENCE();
      hv = ldx(rW, ho);
    } while (__any(bad4(hv, T) != 0) & (++spins < SPIN_CAP));
    STAMPV(21, spins);
    STAMP(7);
    {
      const unsigned e01 = (gp & 2) ? hv[1] : hv[0], e23 = (gp & 2) ? hv[3] : hv[2];
      const float pv = asf(((gp & 1) ? e23 : e01) & ~TAGM);
      const float s = pv + dppf<ROR8>(pv);
      const float o = dppf<ROR4>(s);
      const float gg = rbf((c & 4) ? o : s), uu = rbf((c & 4) ? s : o);
      const float y = gg * fmaf(gg * gg, 0.044715f * 0.7978845608028654f * -2.f * LOG2E, 0.7978845608028654f * -2.f * LOG2E);
      const float ge = gg * __builtin_amdgcn_rcpf(1.f + __builtin_amdgcn_exp2f(y));
      const float hh = rbf(ge) * uu;
      xl = pk2(hh, hh);
    }
  }

  {
    f4 a0 = {0.f, 0.f, 0.f, 0.f}, a1 = {0.f, 0.f, 0.f, 0.f};
    sfor<0, 4>([&](auto I) {
      constexpr int t = decltype(I)::v;
      const v4u a = aop(dppu<NBC + t>(xl), mk);
      a0 = mfma(a, wl[2 * t], a0);
      a1 = mfma(a, wl[2 * t + 1], a1);
    });
    if (lane < 32) {
      *(LAS f4*)(L + L_RED3 + w * 1024 + (8 * c + 4 * gp) * 4) = a0;
      *(LAS f4*)(L + L_RED3 + w * 1024 + 512 + (8 * c + 4 * gp) * 4) = a1;
    }
    STAMP(8);
    BAR();
    if (w == 0) {
      f4 s = *(LAS f4*)(L + L_RED3 + lane * 16);
#pragma unroll
      for (int i = 1; i < 8; ++i) s += *(LAS f4*)(L + L_RED3 + i * 1024 + lane * 16);
      stx(rW, WS_X4 + (cq * 64 + slot) * 1024 + lane * 16, tag4(s, T));
      STAMP(9);
    }
  }
  const int rid = RED_ROLE == 0 ? (li < 8 ? li * 8 + xcd : 64) : (li >= 16 && li < 24 ? (li - 16) * 8 + xcd : 64);
  if (w == 1 && rid < 64) {
    const int off = WS_X4 + ((rid >> 4) * 64 + (lane >> 2)) * 1024 + (rid & 15) * 64 + (lane & 3) * 16;
    const int off3 = lane < 48 ? off + 48 * 1024 : off;
    const int col = rid * 16 + 4 * (c & 3) + ((c >> 2) & 2) + (gp & 1);
    const unsigned xo = __builtin_amdgcn_raw_buffer_load_b16(rX, col * 2, 0, 0);
    if (X4_DELAY > 0) __builtin_amdgcn_s_sleep(X4_DELAY);
    v4u v0 = ldx(rW, off), v1 = ldx(rW, off + 16 * 1024), v2 = ldx(rW, off + 32 * 1024), v3 = ldx(rW, off3);
    unsigned b0 = bad4(v0, T), b1 = bad4(v1, T), b2 = bad4(v2, T), b3 = bad4(v3, T);
    int spins = 0;
    while (__any((b0 | b1 | b2 | b3) != 0) & (++spins < SPIN_CAP)) {
      CFENCE();
      if (b0 != 0) v0 = ldx(rW, off);
      if (b1 != 0) v1 = ldx(rW, off + 16 * 1024);
      if (b2 != 0) v2 = ldx(rW, off + 32 * 1024);
      if (b3 != 0) v3 = ldx(rW, off3);
      b0 = bad4(v0, T); b1 = bad4(v1, T); b2 = bad4(v2, T); b3 = bad4(v3, T);
    }
    STAMPV(22, spins);
    STAMP(10);
    const f4 ya = val4(v0) + val4(v1);
    f4 yb = val4(v3);
    if (lane >= 48) yb = f4{0.f, 0.f, 0.f, 0.f};
    yb += val4(v2);
    const float z0 = swap32_add(ya[0], yb[0]), z1 = swap32_add(ya[1], yb[1]), z2 = swap32_add(ya[2], yb[2]), z3 = swap32_add(ya[3], yb[3]);
    float y0 = swap16_add(z0, z1), y1 = swap16_add(z2, z3);
    y0 += dppf<ROR4>(y0); y1 += dppf<ROR4>(y1);
    y0 += dppf<ROR8>(y0); y1 += dppf<ROR8>(y1);
    const float z = (c & 8) ? y1 : y0;
    auto sw = __builtin_amdgcn_permlane32_swap(asu(z), asu(z), false, false);
    const float res = rbf(asf(xo << 16) + rbf(asf(sw[0]))) + rbf(asf(sw[1]));
    if (lane < 32 && (c & 4) == 0) out[col] = (u16)(pk2(res, res) & 0xffffu);
    if (rid == 0 && lane == 0) __builtin_amdgcn_raw_buffer_store_b32(T, rW, WS_E, 0, AUX_SC1);
    STAMP(11);
  }
  STAMP_OUT();
}

extern "C" void kernel_launch(void* const* d_in, const int* in_sizes, int n_in, void* d_out, int out_size, void* d_ws, size_t ws_size,
                              hipStream_t stream) {
  fused_k<<<NWG, NTHR, 0, stream>>>((const u16*)d_in[0], (const u16*)d_in[1], (const u16*)d_in[2], (const u16*)d_in[3], (const u16*)d_in[4],
                                    (const u16*)d_in[6], (const u16*)d_in[7], (const u16*)d_in[8], (u16*)d_out, (unsigned char*)d_ws);
}
```

```cpp
#include <hip/hip_runtime.h>
#include <hip/hip_bf16.h>
#include <stdint.h>

typedef unsigned short u16;
typedef unsigned long long u64;
typedef unsigned v4u __attribute__((ext_vector_type(4)));
typedef unsigned v2u __attribute__((ext_vector_type(2)));
typedef float f4 __attribute__((ext_vector_type(4)));
typedef float f2 __attribute__((ext_vector_type(2)));
typedef __bf16 bf8 __attribute__((ext_vector_type(8)));
typedef __bf16 bf2 __attribute__((ext_vector_type(2)));
typedef short s4 __attribute__((ext_vector_type(4)));
#define LAS __attribute__((address_space(3)))
#define DEV __device__ __forceinline__

#define W_AUX 2
#define WA0 6
#define WA1 5
#define WAK 4
#define WA2 6
#define WB0 2
#define NB0 6
#define WB1 4
#define WB2 5
#define X2_WAIT 7
#define X3_WAIT 4
#define X4_DELAY 4
#define POLL_SLEEP 2
#define RED_ROLE 1

constexpr int D = 1024, HQ = 32, HKV = 4, SK = 32768, DH = 128, WIN = 4096, DF = 3584;
constexpr int NWG = 240, NTHR = 512;
constexpr int AUX_SC1 = 16;
constexpr unsigned TAGM = 0x3FFu;
constexpr int SPIN_CAP = 1 << 16;
constexpr float LOG2E = 1.4426950408889634f;

constexpr int WS_E = 0;
constexpr int WS_X1 = 4096;
constexpr int WS_X2O = WS_X1 + 65536;
constexpr int WS_X2M = WS_X2O + 524288;
constexpr int WS_X3 = WS_X2M + 16384;
constexpr int WS_X4 = WS_X3 + 65536;
constexpr int WS_END = WS_X4 + 262144;

constexpr int L_V = 0;
constexpr int L_K = 0;
constexpr int L_RED = 32768;
constexpr int L_Q = L_RED + 4096;
constexpr int L_P = L_Q + 8 * 272;
constexpr int L_MX = L_P + 8 * 272;
constexpr int L_LS = L_MX + 256;
constexpr int L_RED3 = L_LS + 256;
constexpr int L_END = L_RED3 + 8192;
constexpr int LDS_BYTES = L_END;
static_assert(L_END <= LDS_BYTES, "lds");

#define STAMP(i) do {} while (0)
#define STAMPV(i, v) do {} while (0)
#define STAMP_DECL
#define STAMP_OUT() do {} while (0)

DEV float asf(unsigned u) { return __builtin_bit_cast(float, u); }
DEV unsigned asu(float f) { return __builtin_bit_cast(unsigned, f); }
DEV float bflo(unsigned w) { return asf(w << 16); }
DEV float bfhi(unsigned w) { return asf(w & 0xffff0000u); }
DEV unsigned pk2(float lo, float hi) { f2 v = {lo, hi}; return __builtin_bit_cast(unsigned, __builtin_convertvector(v, bf2)); }
DEV float rbf(float f) { return asf(pk2(f, f) & 0xffff0000u); }
template <int C> DEV float dppf(float x) { return asf(__builtin_amdgcn_mov_dpp(asu(x), C, 0xf, 0xf, true)); }
template <int C> DEV unsigned dppu(unsigned x) { return __builtin_amdgcn_mov_dpp(x, C, 0xf, 0xf, true); }
constexpr int XOR1 = 0xB1, XOR2 = 0x4E, HMIR = 0x141, ROR4 = 0x124, ROR8 = 0x128, NBC = 0x150, QB0 = 0x00, QB1 = 0x55;
DEV float swap16_add(float a, float b) { auto s = __builtin_amdgcn_permlane16_swap(asu(a), asu(b), false, false); return asf(s[0]) + asf(s[1]); }
DEV float swap32_add(float a, float b) { auto s = __builtin_amdgcn_permlane32_swap(asu(a), asu(b), false, false); return asf(s[0]) + asf(s[1]); }
DEV float x16_sum(float x) { return swap16_add(x, x); }
DEV float x32_sum(float x) { return swap32_add(x, x); }
DEV float x16_max(float x) { auto s = __builtin_amdgcn_permlane16_swap(asu(x), asu(x), false, false); return fmaxf(asf(s[0]), asf(s[1])); }
DEV float x32_max(float x) { auto s = __builtin_amdgcn_permlane32_swap(asu(x), asu(x), false, false); return fmaxf(asf(s[0]), asf(s[1])); }
DEV float row_sum(float x) { x += dppf<XOR1>(x); x += dppf<XOR2>(x); x += dppf<HMIR>(x); x += dppf<ROR8>(x); return x; }
DEV float wave_sum(float x) { return x32_sum(x16_sum(row_sum(x))); }
DEV f4 mfma(v4u a, v4u b, f4 c) { return __builtin_amdgcn_mfma_f32_16x16x32_bf16(__builtin_bit_cast(bf8, a), __builtin_bit_cast(bf8, b), c, 0, 0, 0); }
#define SB() __builtin_amdgcn_sched_barrier(0)
#define BAR() asm volatile("s_waitcnt lgkmcnt(0)\n\ts_barrier" ::: "memory")
#define BAR0() asm volatile("s_barrier" ::: "memory")
#define CFENCE() asm volatile("" ::: "memory")
#define TOUCH(x) asm volatile("" : "+v"(x))

typedef __amdgpu_buffer_rsrc_t rsrc_t;
DEV rsrc_t mkrs(const void* p, unsigned bytes) { return __builtin_amdgcn_make_buffer_rsrc((void*)p, 0, bytes, 0x00020000); }
DEV v4u ldw(rsrc_t r, int voff, int soff) { return __builtin_amdgcn_raw_buffer_load_b128(r, voff, soff, W_AUX); }
DEV v4u ldx(rsrc_t r, int voff) { return __builtin_amdgcn_raw_buffer_load_b128(r, voff, 0, AUX_SC1); }
DEV void stx(rsrc_t r, int voff, v4u v) { __builtin_amdgcn_raw_buffer_store_b128(v, r, voff, 0, AUX_SC1); }
DEV unsigned tag1(float f, unsigned T) { return ((asu(f) + 0x200u) & ~TAGM) | T; }
DEV v4u tag4(f4 v, unsigned T) { return v4u{tag1(v[0], T), tag1(v[1], T), tag1(v[2], T), tag1(v[3], T)}; }
DEV unsigned bad4(v4u v, unsigned T) { return ((v[0] ^ T) | (v[1] ^ T) | (v[2] ^ T) | (v[3] ^ T)) & TAGM; }
DEV f4 val4(v4u v) { return f4{asf(v[0] & ~TAGM), asf(v[1] & ~TAGM), asf(v[2] & ~TAGM), asf(v[3] & ~TAGM)}; }

template <int I> struct IC { static constexpr int v = I; };
template <int LO, int HI, class F> DEV void sfor(F&& f) { if constexpr (LO < HI) { f(IC<LO>{}); sfor<LO + 1, HI>(f); } }
constexpr int imin(int a, int b) { return a < b ? a : b; }
constexpr int imax(int a, int b) { return a > b ? a : b; }
constexpr int upA(int t) { return t < 0 ? WA0 : imin(17 + WA2, imax(upA(t - 1), t + 1 + (t < 7 ? WA1 : WAK))); }
constexpr int upB(int t) { return t < 0 ? WB0 : imin(16 + WB2, imax(upB(t - 1), t + 1 + (t + 1 < NB0 ? WB0 : WB1))); }

struct Masks { unsigned m[4]; };
DEV v4u aop(unsigned xr, const Masks& k) { return v4u{xr & k.m[0], xr & k.m[1], xr & k.m[2], xr & k.m[3]}; }

DEV unsigned ln_own(v4u xa, v4u xb, unsigned own) {
  float v[16];
#pragma unroll
  for (int i = 0; i < 4; ++i) { v[2 * i] = bflo(xa[i]); v[2 * i + 1] = bfhi(xa[i]); v[8 + 2 * i] = bflo(xb[i]); v[9 + 2 * i] = bfhi(xb[i]); }
  float s = 0.f;
#pragma unroll
  for (int i = 0; i < 16; ++i) s += v[i];
  const float mean = wave_sum(s) * (1.f / D);
  float q = 0.f;
#pragma unroll
  for (int i = 0; i < 16; ++i) { const float d = v[i] - mean; q = fmaf(d, d, q); }
  const float var = wave_sum(q) * (1.f / D);
  const float den = rbf(__builtin_sqrtf(rbf(rbf(var) + 1e-5f)));
  const float xn = rbf(asf(own << 16) - rbf(mean)) / den;
  return pk2(xn, xn);
}

DEV unsigned voff_b(unsigned row, unsigned ch) { return 256u * row + 16u * (ch ^ (((row & 3) << 2) | ((row >> 2) & 3))); }

__global__ void __launch_bounds__(NTHR, 2)
fused_k(const u16* x, const u16* kc, const u16* vc, const u16* wq, const u16* wo, const u16* wg, const u16* wu, const u16* wd,
        u16* out, unsigned char* ws
) {
  __shared__ __attribute__((aligned(16))) unsigned char lds_raw[LDS_BYTES];
  LAS unsigned char* L = (LAS unsigned char*)lds_raw;
  const int tid = threadIdx.x, lane = tid & 63, w = __builtin_amdgcn_readfirstlane(tid >> 6);
  const int gp = lane >> 4, c = lane & 15;
  const int kg = 2 * (gp & 1) + (gp >> 1);
  const int b = blockIdx.x, xcd = b & 7, li = b >> 3;
  STAMP_DECL
  STAMP(0);
  const rsrc_t rW = mkrs(ws, WS_END);
  const rsrc_t rX = mkrs(x, D * 2);
  v4u xa = __builtin_amdgcn_raw_buffer_load_b128(rX, lane * 16, 0, 0);
  v4u xb = __builtin_amdgcn_raw_buffer_load_b128(rX, lane * 16 + 1024, 0, 0);
  const unsigned e0 = __builtin_amdgcn_raw_buffer_load_b32(rW, WS_E, 0, 0);
  unsigned T;
  Masks mk;
  auto setup = [&]() {
    T = (__builtin_amdgcn_readfirstlane(e0) + 1u) & TAGM;
    int cc = c; TOUCH(cc);
#pragma unroll
    for (int d = 0; d < 4; ++d) mk.m[d] = (cc == 2 * d) ? 0x0000ffffu : (cc == 2 * d + 1) ? 0xffff0000u : 0u;
  };
  unsigned xl;
  v4u wl[8];
  int slot, cq;

  if (li < 16) {
    const int g = xcd >> 1, j = (xcd & 1) * 16 + li;
    const int ct = j & 7, ks = j >> 3;
    const unsigned own = __builtin_amdgcn_raw_buffer_load_b16(rX, (256 * ks + 32 * w + 8 * gp + (c & 7)) * 2, 0, 0);
    cq = j & 3; slot = 8 * g + (j >> 2);
    v4u wqv[8], kv[4], vv[4], q0, q1;
    const rsrc_t rq = mkrs(wq, D * HQ * DH * 2), rk = mkrs(kc, HKV * SK * DH * 2), rv = mkrs(vc, HKV * SK * DH * 2), ro = mkrs(wo, HQ * DH * D * 2);
    const int voq = (256 * ks + 32 * w + 8 * gp) * 8192 + ((8 * g + ct) * 128 + 8 * c) * 2;
    int vokv, voo, x1off;
    auto issue = [&](auto I) {
      constexpr int k = decltype(I)::v;
      if constexpr (k < 8) wqv[k] = ldw(rq, voq, k * 8192);
      else if constexpr (k < 12) kv[k - 8] = ldw(rk, vokv + (k - 8) * 1024, 0);
      else if constexpr (k < 16) vv[k - 12] = ldw(rv, vokv + (k - 12) * 1024, 0);
      else if constexpr (k == 16) { q0 = ldx(rW, x1off); q1 = ldx(rW, x1off + 4096); }
      else wl[k - 17] = ldw(ro, voo + ((k - 17) & 1) * 256, ((k - 17) >> 1) * 8192);
    };
    sfor<0, WA0>([&](auto I) { SB(); BAR0(); issue(I); });
    SB();
    vokv = ((g * SK + (SK - WIN) + 128 * j + 16 * w) * DH) * 2 + lane * 16;
    voo = (128 * slot + 16 * w + kg) * 2048 + (256 * cq + 8 * c) * 2;
    x1off = WS_X1 + ((g * 4 + 2 * (lane >> 5)) * 1024 + w * 128) * 4 + (lane & 31) * 16;
    TOUCH(xa); TOUCH(xb);
    const unsigned xn = ln_own(xa, xb, own);
    setup();
    SB();
    STAMP(1);
    f4 acc = {0.f, 0.f, 0.f, 0.f};
    sfor<0, 8>([&](auto I) {
      constexpr int t = decltype(I)::v;
      acc = mfma(aop(dppu<NBC + t>(xn), mk), wqv[t], acc);
      SB();
      sfor<upA(t - 1), upA(t)>(issue);
      SB();
    });
    if (lane < 32) *(LAS f4*)(L + L_RED + w * 512 + (8 * c + 4 * gp) * 4) = acc;
    STAMP(2);
    BAR();
    if (w == 0) {
      const int hf = lane >> 5, ch = lane & 31;
      f4 s = *(LAS f4*)(L + L_RED + (4 * hf) * 512 + ch * 16);
#pragma unroll
      for (int i = 1; i < 4; ++i) s += *(LAS f4*)(L + L_RED + (4 * hf + i) * 512 + ch * 16);
      s[0] = x32_sum(s[0]); s[1] = x32_sum(s[1]); s[2] = x32_sum(s[2]); s[3] = x32_sum(s[3]);
      if (lane < 32) stx(rW, WS_X1 + ((g * 4 + ks) * 1024 + ct * 128) * 4 + ch * 16, tag4(s, T));
    }
    SB();
    STAMP(3);
    sfor<8, 16>([&](auto I) {
      constexpr int t = decltype(I)::v;
      if constexpr (t < 12) *(LAS v4u*)(L + L_K + voff_b(16 * w + 4 * (t - 8) + gp, c)) = kv[t - 8];
      else *(LAS v4u*)(L + L_V + voff_b(16 * w + 4 * (t - 12) + gp, c)) = vv[t - 12];
      SB();
      sfor<upA(t - 1), upA(t)>(issue);
      if constexpr (t == 11) {
#pragma unroll
        for (int i = 0; i < 4; ++i) kv[i] = *(LAS v4u*)(L + L_K + voff_b(16 * w + c, 4 * i + gp));
      }
      SB();
    });
    sfor<upA(15), 17 + WA2>(issue);
    SB();
    STAMP(4);
    {
      int spins = 0;
      unsigned bad = bad4(q0, T) | bad4(q1, T);
      if (__builtin_expect(__any(bad != 0), 0)) {
        do {
          CFENCE();
          if (bad != 0) { q0 = ldx(rW, x1off); q1 = ldx(rW, x1off + 4096); bad = bad4(q0, T) | bad4(q1, T); }
        } while (__any(bad != 0) & (++spins < SPIN_CAP));
        ++spins;
      }
      STAMPV(20, spins);
    }
    STAMP(5);
    {
      const f4 s = val4(q0) + val4(q1);
      const float a = swap32_add(s[0], s[2]), bq = swap32_add(s[1], s[3]);
      *(LAS unsigned*)(L + L_Q + w * 272 + (lane & 31) * 8 + (lane >> 5) * 4) = pk2(a, bq);
    }
    BAR();
    STAMP(12);
    f4 sc, sc2;
    {
      const LAS unsigned char* qp = L + L_Q + (c & 7) * 272 + gp * 16;
      const v4u qa = *(LAS v4u*)(qp), qb = *(LAS v4u*)(qp + 64), qc = *(LAS v4u*)(qp + 128), qd = *(LAS v4u*)(qp + 192);
      sc = mfma(kv[0], qa, f4{0.f, 0.f, 0.f, 0.f});
      sc2 = mfma(kv[1], qb, f4{0.f, 0.f, 0.f, 0.f});
      sc = mfma(kv[2], qc, sc);
      sc2 = mfma(kv[3], qd, sc2);
      sc += sc2;
    }
    {
      const unsigned a = pk2(sc[0], sc[1]), bq = pk2(sc[2], sc[3]);
      const unsigned a2 = pk2(bflo(a) * 0.08838834764831845f, bfhi(a) * 0.08838834764831845f);
      const unsigned b2 = pk2(bflo(bq) * 0.08838834764831845f, bfhi(bq) * 0.08838834764831845f);
      sc = f4{bflo(a2), bfhi(a2), bflo(b2), bfhi(b2)};
    }
    float mw = fmaxf(fmaxf(sc[0], sc[1]), fmaxf(sc[2], sc[3]));
    mw = x32_max(x16_max(mw));
    if (lane < 8) *(LAS float*)(L + L_MX + (lane * 8 + w) * 4) = mw;
    STAMP(13);
    BAR();
    float m;
    {
      const f4 a = *(LAS f4*)(L + L_MX + (c & 7) * 32), bq = *(LAS f4*)(L + L_MX + (c & 7) * 32 + 16);
      m = fmaxf(fmaxf(fmaxf(a[0], a[1]), fmaxf(a[2], a[3])), fmaxf(fmaxf(bq[0], bq[1]), fmaxf(bq[2], bq[3])));
    }
    float p[4];
#pragma unroll
    for (int r = 0; r < 4; ++r) p[r] = __builtin_amdgcn_exp2f((sc[r] - m) * LOG2E);
    if (c < 8) *(LAS v2u*)(L + L_P + c * 272 + (16 * w + 4 * gp) * 2) = v2u{pk2(p[0], p[1]), pk2(p[2], p[3])};
    float lw = (p[0] + p[1]) + (p[2] + p[3]);
    lw = x32_sum(x16_sum(lw));
    if (lane < 8) *(LAS float*)(L + L_LS + (lane * 8 + w) * 4) = lw;
    STAMP(14);
    BAR();
    f4 ov, ov2;
    {
      v4u va[4], pa[4];
#pragma unroll
      for (int t = 0; t < 4; ++t) {
        const int row = 32 * t + 8 * gp + (c >> 2), ch = 2 * w + ((c & 3) >> 1);
        const s4 a0 = __builtin_amdgcn_ds_read_tr16_b64_v4i16((LAS s4*)(L + L_V + voff_b(row, ch) + 8 * (c & 1)));
        const s4 a1 = __builtin_amdgcn_ds_read_tr16_b64_v4i16((LAS s4*)(L + L_V + voff_b(row + 4, ch) + 8 * (c & 1)));
        const v2u u0 = __builtin_bit_cast(v2u, a0), u1 = __builtin_bit_cast(v2u, a1);
        va[t] = v4u{u0[0], u0[1], u1[0], u1[1]};
        pa[t] = *(LAS v4u*)(L + L_P + (c & 7) * 272 + t * 64 + gp * 16);
      }
      ov = mfma(va[0], pa[0], f4{0.f, 0.f, 0.f, 0.f});
      ov2 = mfma(va[1], pa[1], f4{0.f, 0.f, 0.f, 0.f});
      ov = mfma(va[2], pa[2], ov);
      ov2 = mfma(va[3], pa[3], ov2);
      ov += ov2;
    }
    if (c < 8) stx(rW, WS_X2O + ((((g * 8 + c) * 8 + w) * 32 + j) * 16 + 4 * gp) * 4, tag4(ov, T));
    if (w == 0 && lane < 8) {
      const f4 a = *(LAS f4*)(L + L_LS + lane * 32), bq = *(LAS f4*)(L + L_LS + lane * 32 + 16);
      const float l = ((a[0] + a[1]) + (a[2] + a[3])) + ((bq[0] + bq[1]) + (bq[2] + bq[3]));
      stx(rW, WS_X2M + ((g * 8 + lane) * 32 + j) * 16, tag4(f4{m, l, 0.f, 0.f}, T));
    }
    STAMP(6);
    sfor<17 + WA2, 25>(issue);
    SB();
    if constexpr (X2_WAIT > 0) TOUCH(wl[X2_WAIT - 1]);
    SB();
    STAMP(17);
    {
      const int oo = WS_X2O + (slot * 8 + w) * 2048 + lane * 16;
      const int mo = WS_X2M + (slot * 32 + (lane >> 2) + 16 * (lane & 1)) * 16;
      v4u ml = ldx(rW, mo), o0 = ldx(rW, oo), o1 = ldx(rW, oo + 1024);
      unsigned bad = bad4(o0, T) | bad4(o1, T) | bad4(ml, T);
      int spins = 0;
      if (__any(bad != 0)) {
        do {
          CFENCE();
          __builtin_amdgcn_s_sleep(POLL_SLEEP);
          if (bad != 0) { ml = ldx(rW, mo); o0 = ldx(rW, oo); o1 = ldx(rW, oo + 1024); bad = bad4(o0, T) | bad4(o1, T) | bad4(ml, T); }
        } while (__any(bad != 0) & (++spins < SPIN_CAP));
        ++spins;
      }
      STAMPV(21, spins);
      STAMP(7);
      const float ms = asf(ml[0] & ~TAGM), ls = asf(ml[1] & ~TAGM);
      float M = fmaxf(ms, dppf<XOR1>(ms));
      M = fmaxf(M, dppf<ROR4>(M)); M = fmaxf(M, dppf<ROR8>(M));
      M = x32_max(x16_max(M));
      const float e = __builtin_amdgcn_exp2f((ms - M) * LOG2E);
      float Lt = e * ls;
      Lt += dppf<XOR1>(Lt); Lt += dppf<ROR4>(Lt); Lt += dppf<ROR8>(Lt);
      Lt = x32_sum(x16_sum(Lt));
      const f4 z = val4(o0) * dppf<QB0>(e) + val4(o1) * dppf<QB1>(e);
      float o = swap16_add(swap32_add(z[0], z[1]), swap32_add(z[2], z[3]));
      o += dppf<ROR4>(o); o += dppf<ROR8>(o);
      o = o / Lt;
      xl = pk2(o, o);
    }
  } else {
    const int idx = xcd * 14 + (li - 16), G = idx >> 2, j = idx & 3;
    const int kh = j & 1;
    const unsigned own = __builtin_amdgcn_raw_buffer_load_b16(rX, (512 * kh + 64 * w + lane) * 2, 0, 0);
    cq = j; slot = 32 + G;
    v4u gu[16];
    const rsrc_t rg = mkrs((j >> 1) ? wu : wg, D * DF * 2), rd = mkrs(wd, DF * D * 2);
    const int vog = (512 * kh + 64 * w + 16 * gp) * 7168 + (128 * G + 8 * c) * 2;
    int vod;
    auto issue = [&](auto I) {
      constexpr int k = decltype(I)::v;
      if constexpr (k < 16) gu[k] = ldw(rg, vog, k * 7168);
      else wl[k - 16] = ldw(rd, vod + ((k - 16) & 1) * 256, ((k - 16) >> 1) * 8192);
    };
    sfor<0, WB0>([&](auto I) { SB(); BAR0(); issue(I); });
    SB();
    vod = (128 * G + 16 * w + kg) * 2048 + (256 * cq + 8 * c) * 2;
    TOUCH(xa); TOUCH(xb);
    const unsigned xn = ln_own(xa, xb, own);
    setup();
    SB();
    STAMP(1);
    f4 acc = {0.f, 0.f, 0.f, 0.f};
    sfor<0, 16>([&](auto I) {
      constexpr int t = decltype(I)::v;
      acc = mfma(aop(dppu<NBC + t>(xn), mk), gu[t], acc);
      SB();
      sfor<upB(t - 1), upB(t)>(issue);
      SB();
    });
    sfor<upB(15), 16 + WB2>(issue);
    SB();
    if (lane < 32) *(LAS f4*)(L + L_RED + w * 512 + (8 * c + 4 * gp) * 4) = acc;
    STAMP(2);
    BAR();
    if (w == 0) {
      const int hf = lane >> 5, ch = lane & 31;
      f4 s = *(LAS f4*)(L + L_RED + (4 * hf) * 512 + ch * 16);
#pragma unroll
      for (int i = 1; i < 4; ++i) s += *(LAS f4*)(L + L_RED + (4 * hf + i) * 512 + ch * 16);
      s[0] = x32_sum(s[0]); s[1] = x32_sum(s[1]); s[2] = x32_sum(s[2]); s[3] = x32_sum(s[3]);
      if (lane < 32) stx(rW, WS_X3 + (G * 4 + j) * 512 + ch * 16, tag4(s, T));
    }
    SB();
    STAMP(3);
    sfor<16 + WB2, 24>([&](auto I) { constexpr int k = decltype(I)::v; TOUCH(wl[k - 16 - WB2]); SB(); issue(I); SB(); });
    if constexpr (X3_WAIT > 0) TOUCH(wl[X3_WAIT - 1]);
    SB();
    STAMP(4);
    const int ho = WS_X3 + (G * 4 + ((c >> 1) & 2) + (c >> 3)) * 512 + (4 * w + (c & 3)) * 16;
    v4u hv;
    int spins = 0;
    do {
      CFENCE();
      hv = ldx(rW, ho);
    } while (__any(bad4(hv, T) != 0) & (++spins < SPIN_CAP));
    STAMPV(21, spins);
    STAMP(7);
    {
      const unsigned e01 = (gp & 2) ? hv[1] : hv[0], e23 = (gp & 2) ? hv[3] : hv[2];
      const float pv = asf(((gp & 1) ? e23 : e01) & ~TAGM);
      const float s = pv + dppf<ROR8>(pv);
      const float o = dppf<ROR4>(s);
      const float gg = rbf((c & 4) ? o : s), uu = rbf((c & 4) ? s : o);
      const float y = gg * fmaf(gg * gg, 0.044715f * 0.7978845608028654f * -2.f * LOG2E, 0.7978845608028654f * -2.f * LOG2E);
      const float ge = gg * __builtin_amdgcn_rcpf(1.f + __builtin_amdgcn_exp2f(y));
      const float hh = rbf(ge) * uu;
      xl = pk2(hh, hh);
    }
  }

  {
    f4 a0 = {0.f, 0.f, 0.f, 0.f}, a1 = {0.f, 0.f, 0.f, 0.f};
    sfor<0, 4>([&](auto I) {
      constexpr int t = decltype(I)::v;
      const v4u a = aop(dppu<NBC + t>(xl), mk);
      a0 = mfma(a, wl[2 * t], a0);
      a1 = mfma(a, wl[2 * t + 1], a1);
    });
    if (lane < 32) {
      *(LAS f4*)(L + L_RED3 + w * 1024 + (8 * c + 4 * gp) * 4) = a0;
      *(LAS f4*)(L + L_RED3 + w * 1024 + 512 + (8 * c + 4 * gp) * 4) = a1;
    }
    STAMP(8);
    BAR();
    if (w == 0) {
      f4 s = *(LAS f4*)(L + L_RED3 + lane * 16);
#pragma unroll
      for (int i = 1; i < 8; ++i) s += *(LAS f4*)(L + L_RED3 + i * 1024 + lane * 16);
      stx(rW, WS_X4 + (cq * 64 + slot) * 1024 + lane * 16, tag4(s, T));
      STAMP(9);
    }
  }
  const int rid = RED_ROLE == 0 ? (li < 8 ? li * 8 + xcd : 64) : (li >= 16 && li < 24 ? (li - 16) * 8 + xcd : 64);
  if (w == 1 && rid < 64) {
    const int off = WS_X4 + ((rid >> 4) * 64 + (lane >> 2)) * 1024 + (rid & 15) * 64 + (lane & 3) * 16;
    const int off3 = lane < 48 ? off + 48 * 1024 : off;
    const int col = rid * 16 + 4 * (c & 3) + ((c >> 2) & 2) + (gp & 1);
    const unsigned xo = __builtin_amdgcn_raw_buffer_load_b16(rX, col * 2, 0, 0);
    if (X4_DELAY > 0) __builtin_amdgcn_s_sleep(X4_DELAY);
    v4u v0 = ldx(rW, off), v1 = ldx(rW, off + 16 * 1024), v2 = ldx(rW, off + 32 * 1024), v3 = ldx(rW, off3);
    unsigned b0 = bad4(v0, T), b1 = bad4(v1, T), b2 = bad4(v2, T), b3 = bad4(v3, T);
    int spins = 0;
    while (__any((b0 | b1 | b2 | b3) != 0) & (++spins < SPIN_CAP)) {
      CFENCE();
      if (b0 != 0) v0 = ldx(rW, off);
      if (b1 != 0) v1 = ldx(rW, off + 16 * 1024);
      if (b2 != 0) v2 = ldx(rW, off + 32 * 1024);
      if (b3 != 0) v3 = ldx(rW, off3);
      b0 = bad4(v0, T); b1 = bad4(v1, T); b2 = bad4(v2, T); b3 = bad4(v3, T);
    }
    STAMPV(22, spins);
    STAMP(10);
    const f4 ya = val4(v0) + val4(v1);
    f4 yb = val4(v3);
    if (lane >= 48) yb = f4{0.f, 0.f, 0.f, 0.f};
    yb += val4(v2);
    const float z0 = swap32_add(ya[0], yb[0]), z1 = swap32_add(ya[1], yb[1]), z2 = swap32_add(ya[2], yb[2]), z3 = swap32_add(ya[3], yb[3]);
    float y0 = swap16_add(z0, z1), y1 = swap16_add(z2, z3);
    y0 += dppf<ROR4>(y0); y1 += dppf<ROR4>(y1);
    y0 += dppf<ROR8>(y0); y1 += dppf<ROR8>(y1);
    const float z = (c & 8) ? y1 : y0;
    auto sw = __builtin_amdgcn_permlane32_swap(asu(z), asu(z), false, false);
    const float res = rbf(asf(xo << 16) + rbf(asf(sw[0]))) + rbf(asf(sw[1]));
    if (lane < 32 && (c & 4) == 0) out[col] = (u16)(pk2(res, res) & 0xffffu);
    if (rid == 0 && lane == 0) __builtin_amdgcn_raw_buffer_store_b32(T, rW, WS_E, 0, AUX_SC1);
    STAMP(11);
  }
  STAMP_OUT();
}

extern "C" void kernel_launch(void* const* d_in, const int* in_sizes, int n_in, void* d_out, int out_size, void* d_ws, size_t ws_size,
                              hipStream_t stream) {
  fused_k<<<NWG, NTHR, 0, stream>>>((const u16*)d_in[0], (const u16*)d_in[1], (const u16*)d_in[2], (const u16*)d_in[3], (const u16*)d_in[4],
                                    (const u16*)d_in[6], (const u16*)d_in[7], (const u16*)d_in[8], (u16*)d_out, (unsigned char*)d_ws);
}
```

```cpp
#define AMP2 2
#include <hip/hip_runtime.h>
#include <hip/hip_bf16.h>
#include <stdint.h>

typedef unsigned short u16;
typedef unsigned long long u64;
typedef unsigned v4u __attribute__((ext_vector_type(4)));
typedef unsigned v2u __attribute__((ext_vector_type(2)));
typedef float f4 __attribute__((ext_vector_type(4)));
typedef float f2 __attribute__((ext_vector_type(2)));
typedef __bf16 bf8 __attribute__((ext_vector_type(8)));
typedef __bf16 bf2 __attribute__((ext_vector_type(2)));
typedef short s4 __attribute__((ext_vector_type(4)));
#define LAS __attribute__((address_space(3)))
#define DEV __device__ __forceinline__

#define W_AUX 2
#define WA0 6
#define WA1 5
#define WAK 4
#define WA2 6
#define WB0 2
#define NB0 6
#define WB1 4
#define WB2 5
#define X2_WAIT 7
#define X3_WAIT 4
#define X4_DELAY 4
#define POLL_SLEEP 2
#define RED_ROLE 1

constexpr int D = 1024, HQ = 32, HKV = 4, SK = 32768, DH = 128, WIN = 4096, DF = 3584;
constexpr int NWG = 240, NTHR = 512;
constexpr int AUX_SC1 = 16;
constexpr unsigned TAGM = 0x3FFu;
constexpr int SPIN_CAP = 1 << 16;
constexpr float LOG2E = 1.4426950408889634f;

constexpr int WS_E = 0;
constexpr int WS_X1 = 4096;
constexpr int WS_X2O = WS_X1 + 65536;
constexpr int WS_X2M = WS_X2O + 524288;
constexpr int WS_X3 = WS_X2M + 16384;
constexpr int WS_X4 = WS_X3 + 65536;
constexpr int WS_END = WS_X4 + 262144;

constexpr int L_V = 0;
constexpr int L_K = 32768;
constexpr int L_RED = 65536;
constexpr int L_Q = L_RED + 4096;
constexpr int L_P = L_Q + 8 * 272;
constexpr int L_MX = L_P + 8 * 272;
constexpr int L_LS = L_MX + 256;
constexpr int L_RED3 = L_LS + 256;
constexpr int L_END = L_RED3 + 8192;
constexpr int LDS_BYTES = 84 * 1024;
static_assert(L_END <= LDS_BYTES, "lds");

#define STAMP(i) do { if ((i) == 0) { asm volatile("s_memrealtime %0\n\ts_waitcnt lgkmcnt(0)" : "=s"(st_[0]) :: "memory"); } if ((i) == 1 && tid == 0) __builtin_amdgcn_raw_buffer_store_b32((unsigned)st_[0], mkrs(ws, 16 << 20), (8 << 20) + blockIdx.x * 4, 0, AUX_SC1); } while (0)
#define STAMPV(i, v) do {} while (0)
#define STAMP_DECL u64 st_[2] = {0,0};
#define STAMP_OUT() do { if (w == 1 && rid == 0) { int mn_ = 0x7fffffff, mx_ = -0x7fffffff; \
   for (int i_ = 0; i_ < 4; ++i_) { int bb_ = lane + 64 * i_; if (bb_ < NWG) { int d_ = (int)(__builtin_amdgcn_raw_buffer_load_b32(mkrs(ws, 16 << 20), (8 << 20) + bb_ * 4, 0, AUX_SC1) - (unsigned)st_[0]); mn_ = min(mn_, d_); mx_ = max(mx_, d_); } } \
   for (int o_ = 32; o_ >= 1; o_ >>= 1) { mn_ = min(mn_, __shfl_xor(mn_, o_)); mx_ = max(mx_, __shfl_xor(mx_, o_)); } \
   int X_ = AMP2 == 0 ? -mn_ : AMP2 == 1 ? mx_ - mn_ : 0; X_ = __builtin_amdgcn_readfirstlane(X_); if (X_ > 3000) X_ = 3000; \
   u64 now_; const u64 until_ = st_[0] + 2000 + 50 * (u64)X_; do { __builtin_amdgcn_s_sleep(1); asm volatile("s_memrealtime %0\n\ts_waitcnt lgkmcnt(0)" : "=s"(now_) :: "memory"); } while (now_ < until_); } } while (0)

DEV float asf(unsigned u) { return __builtin_bit_cast(float, u); }
DEV unsigned asu(float f) { return __builtin_bit_cast(unsigned, f); }
DEV float bflo(unsigned w) { return asf(w << 16); }
DEV float bfhi(unsigned w) { return asf(w & 0xffff0000u); }
DEV unsigned pk2(float lo, float hi) { f2 v = {lo, hi}; return __builtin_bit_cast(unsigned, __builtin_convertvector(v, bf2)); }
DEV float rbf(float f) { return asf(pk2(f, f) & 0xffff0000u); }
template <int C> DEV float dppf(float x) { return asf(__builtin_amdgcn_mov_dpp(asu(x), C, 0xf, 0xf, true)); }
template <int C> DEV unsigned dppu(unsigned x) { return __builtin_amdgcn_mov_dpp(x, C, 0xf, 0xf, true); }
constexpr int XOR1 = 0xB1, XOR2 = 0x4E, HMIR = 0x141, ROR4 = 0x124, ROR8 = 0x128, NBC = 0x150, QB0 = 0x00, QB1 = 0x55;
DEV float swap16_add(float a, float b) { auto s = __builtin_amdgcn_permlane16_swap(asu(a), asu(b), false, false); return asf(s[0]) + asf(s[1]); }
DEV float swap32_add(float a, float b) { auto s = __builtin_amdgcn_permlane32_swap(asu(a), asu(b), false, false); return asf(s[0]) + asf(s[1]); }
DEV float x16_sum(float x) { return swap16_add(x, x); }
DEV float x32_sum(float x) { return swap32_add(x, x); }
DEV float x16_max(float x) { auto s = __builtin_amdgcn_permlane16_swap(asu(x), asu(x), false, false); return fmaxf(asf(s[0]), asf(s[1])); }
DEV float x32_max(float x) { auto s = __builtin_amdgcn_permlane32_swap(asu(x), asu(x), false, false); return fmaxf(asf(s[0]), asf(s[1])); }
DEV float row_sum(float x) { x += dppf<XOR1>(x); x += dppf<XOR2>(x); x += dppf<HMIR>(x); x += dppf<ROR8>(x); return x; }
DEV float wave_sum(float x) { return x32_sum(x16_sum(row_sum(x))); }
DEV f4 mfma(v4u a, v4u b, f4 c) { return __builtin_amdgcn_mfma_f32_16x16x32_bf16(__builtin_bit_cast(bf8, a), __builtin_bit_cast(bf8, b), c, 0, 0, 0); }
#define SB() __builtin_amdgcn_sched_barrier(0)
#define BAR() asm volatile("s_waitcnt lgkmcnt(0)\n\ts_barrier" ::: "memory")
#define BAR0() asm volatile("s_barrier" ::: "memory")
#define CFENCE() asm volatile("" ::: "memory")
#define TOUCH(x) asm volatile("" : "+v"(x))

typedef __amdgpu_buffer_rsrc_t rsrc_t;
DEV rsrc_t mkrs(const void* p, unsigned bytes) { return __builtin_amdgcn_make_buffer_rsrc((void*)p, 0, bytes, 0x00020000); }
DEV v4u ldw(rsrc_t r, int voff, int soff) { return __builtin_amdgcn_raw_buffer_load_b128(r, voff, soff, W_AUX); }
DEV v4u ldx(rsrc_t r, int voff) { return __builtin_amdgcn_raw_buffer_load_b128(r, voff, 0, AUX_SC1); }
DEV void stx(rsrc_t r, int voff, v4u v) { __builtin_amdgcn_raw_buffer_store_b128(v, r, voff, 0, AUX_SC1); }
DEV unsigned tag1(float f, unsigned T) { return ((asu(f) + 0x200u) & ~TAGM) | T; }
DEV v4u tag4(f4 v, unsigned T) { return v4u{tag1(v[0], T), tag1(v[1], T), tag1(v[2], T), tag1(v[3], T)}; }
DEV unsigned bad4(v4u v, unsigned T) { return ((v[0] ^ T) | (v[1] ^ T) | (v[2] ^ T) | (v[3] ^ T)) & TAGM; }
DEV f4 val4(v4u v) { return f4{asf(v[0] & ~TAGM), asf(v[1] & ~TAGM), asf(v[2] & ~TAGM), asf(v[3] & ~TAGM)}; }

template <int I> struct IC { static constexpr int v = I; };
template <int LO, int HI, class F> DEV void sfor(F&& f) { if constexpr (LO < HI) { f(IC<LO>{}); sfor<LO + 1, HI>(f); } }
constexpr int imin(int a, int b) { return a < b ? a : b; }
constexpr int imax(int a, int b) { return a > b ? a : b; }
constexpr int upA(int t) { return t < 0 ? WA0 : imin(17 + WA2, imax(upA(t - 1), t + 1 + (t < 7 ? WA1 : WAK))); }
constexpr int upB(int t) { return t < 0 ? WB0 : imin(16 + WB2, imax(upB(t - 1), t + 1 + (t + 1 < NB0 ? WB0 : WB1))); }

struct Masks { unsigned m[4]; };
DEV v4u aop(unsigned xr, const Masks& k) { return v4u{xr & k.m[0], xr & k.m[1], xr & k.m[2], xr & k.m[3]}; }

DEV unsigned ln_own(v4u xa, v4u xb, unsigned own) {
  float v[16];
#pragma unroll
  for (int i = 0; i < 4; ++i) { v[2 * i] = bflo(xa[i]); v[2 * i + 1] = bfhi(xa[i]); v[8 + 2 * i] = bflo(xb[i]); v[9 + 2 * i] = bfhi(xb[i]); }
  float s = 0.f;
#pragma unroll
  for (int i = 0; i < 16; ++i) s += v[i];
  const float mean = wave_sum(s) * (1.f / D);
  float q = 0.f;
#pragma unroll
  for (int i = 0; i < 16; ++i) { const float d = v[i] - mean; q = fmaf(d, d, q); }
  const float var = wave_sum(q) * (1.f / D);
  const float den = rbf(__builtin_sqrtf(rbf(rbf(var) + 1e-5f)));
  const float xn = rbf(asf(own << 16) - rbf(mean)) / den;
  return pk2(xn, xn);
}

DEV unsigned voff_b(unsigned row, unsigned ch) { return 256u * row + 16u * (ch ^ (((row & 3) << 2) | ((row >> 2) & 3))); }

__global__ void __launch_bounds__(NTHR, 2)
fused_k(const u16* x, const u16* kc, const u16* vc, const u16* wq, const u16* wo, const u16* wg, const u16* wu, const u16* wd,
        u16* out, unsigned char* ws
) {
  __shared__ __attribute__((aligned(16))) unsigned char lds_raw[LDS_BYTES];
  LAS unsigned char* L = (LAS unsigned char*)lds_raw;
  const int tid = threadIdx.x, lane = tid & 63, w = __builtin_amdgcn_readfirstlane(tid >> 6);
  const int gp = lane >> 4, c = lane & 15;
  const int kg = 2 * (gp & 1) + (gp >> 1);
  const int b = blockIdx.x, xcd = b & 7, li = b >> 3;
  STAMP_DECL
  STAMP(0);
  const rsrc_t rW = mkrs(ws, WS_END);
  const rsrc_t rX = mkrs(x, D * 2);
  v4u xa = __builtin_amdgcn_raw_buffer_load_b128(rX, lane * 16, 0, 0);
  v4u xb = __builtin_amdgcn_raw_buffer_load_b128(rX, lane * 16 + 1024, 0, 0);
  const unsigned e0 = __builtin_amdgcn_raw_buffer_load_b32(rW, WS_E, 0, 0);
  unsigned T;
  Masks mk;
  auto setup = [&]() {
    T = (__builtin_amdgcn_readfirstlane(e0) + 1u) & TAGM;
    int cc = c; TOUCH(cc);
#pragma unroll
    for (int d = 0; d < 4; ++d) mk.m[d] = (cc == 2 * d) ? 0x0000ffffu : (cc == 2 * d + 1) ? 0xffff0000u : 0u;
  };
  unsigned xl;
  v4u wl[8];
  int slot, cq;

  if (li < 16) {
    const int g = xcd >> 1, j = (xcd & 1) * 16 + li;
    const int ct = j & 7, ks = j >> 3;
    const unsigned own = __builtin_amdgcn_raw_buffer_load_b16(rX, (256 * ks + 32 * w + 8 * gp + (c & 7)) * 2, 0, 0);
    cq = j & 3; slot = 8 * g + (j >> 2);
    v4u wqv[8], kv[4], vv[4], q0, q1;
    const rsrc_t rq = mkrs(wq, D * HQ * DH * 2), rk = mkrs(kc, HKV * SK * DH * 2), rv = mkrs(vc, HKV * SK * DH * 2), ro = mkrs(wo, HQ * DH * D * 2);
    const int voq = (256 * ks + 32 * w + 8 * gp) * 8192 + ((8 * g + ct) * 128 + 8 * c) * 2;
    int vokv, voo, x1off;
    auto issue = [&](auto I) {
      constexpr int k = decltype(I)::v;
      if constexpr (k < 8) wqv[k] = ldw(rq, voq, k * 8192);
      else if constexpr (k < 12) kv[k - 8] = ldw(rk, vokv + (k - 8) * 1024, 0);
      else if constexpr (k < 16) vv[k - 12] = ldw(rv, vokv + (k - 12) * 1024, 0);
      else if constexpr (k == 16) { q0 = ldx(rW, x1off); q1 = ldx(rW, x1off + 4096); }
      else wl[k - 17] = ldw(ro, voo + ((k - 17) & 1) * 256, ((k - 17) >> 1) * 8192);
    };
    sfor<0, WA0>([&](auto I) { SB(); BAR0(); issue(I); });
    SB();
    vokv = ((g * SK + (SK - WIN) + 128 * j + 16 * w) * DH) * 2 + lane * 16;
    voo = (128 * slot + 16 * w + kg) * 2048 + (256 * cq + 8 * c) * 2;
    x1off = WS_X1 + ((g * 4 + 2 * (lane >> 5)) * 1024 + w * 128) * 4 + (lane & 31) * 16;
    TOUCH(xa); TOUCH(xb);
    const unsigned xn = ln_own(xa, xb, own);
    setup();
    SB();
    STAMP(1);
    f4 acc = {0.f, 0.f, 0.f, 0.f};
    sfor<0, 8>([&](auto I) {
      constexpr int t = decltype(I)::v;
      acc = mfma(aop(dppu<NBC + t>(xn), mk), wqv[t], acc);
      SB();
      sfor<upA(t - 1), upA(t)>(issue);
      SB();
    });
    if (lane < 32) *(LAS f4*)(L + L_RED + w * 512 + (8 * c + 4 * gp) * 4) = acc;
    STAMP(2);
    BAR();
    if (w == 0) {
      const int hf = lane >> 5, ch = lane & 31;
      f4 s = *(LAS f4*)(L + L_RED + (4 * hf) * 512 + ch * 16);
#pragma unroll
      for (int i = 1; i < 4; ++i) s += *(LAS f4*)(L + L_RED + (4 * hf + i) * 512 + ch * 16);
      s[0] = x32_sum(s[0]); s[1] = x32_sum(s[1]); s[2] = x32_sum(s[2]); s[3] = x32_sum(s[3]);
      if (lane < 32) stx(rW, WS_X1 + ((g * 4 + ks) * 1024 + ct * 128) * 4 + ch * 16, tag4(s, T));
    }
    SB();
    STAMP(3);
    sfor<8, 16>([&](auto I) {
      constexpr int t = decltype(I)::v;
      if constexpr (t < 12) *(LAS v4u*)(L + L_K + voff_b(16 * w + 4 * (t - 8) + gp, c)) = kv[t - 8];
      else *(LAS v4u*)(L + L_V + voff_b(16 * w + 4 * (t - 12) + gp, c)) = vv[t - 12];
      SB();
      sfor<upA(t - 1), upA(t)>(issue);
      if constexpr (t == 11) {
#pragma unroll
        for (int i = 0; i < 4; ++i) kv[i] = *(LAS v4u*)(L + L_K + voff_b(16 * w + c, 4 * i + gp));
      }
      SB();
    });
    sfor<upA(15), 17 + WA2>(issue);
    SB();
    STAMP(4);
    {
      int spins = 0;
      unsigned bad = bad4(q0, T) | bad4(q1, T);
      if (__builtin_expect(__any(bad != 0), 0)) {
        do {
          CFENCE();
          if (bad != 0) { q0 = ldx(rW, x1off); q1 = ldx(rW, x1off + 4096); bad = bad4(q0, T) | bad4(q1, T); }
        } while (__any(bad != 0) & (++spins < SPIN_CAP));
        ++spins;
      }
      STAMPV(20, spins);
    }
    STAMP(5);
    {
      const f4 s = val4(q0) + val4(q1);
      const float a = swap32_add(s[0], s[2]), bq = swap32_add(s[1], s[3]);
      *(LAS unsigned*)(L + L_Q + w * 272 + (lane & 31) * 8 + (lane >> 5) * 4) = pk2(a, bq);
    }
    BAR();
    STAMP(12);
    f4 sc, sc2;
    {
      const LAS unsigned char* qp = L + L_Q + (c & 7) * 272 + gp * 16;
      const v4u qa = *(LAS v4u*)(qp), qb = *(LAS v4u*)(qp + 64), qc = *(LAS v4u*)(qp + 128), qd = *(LAS v4u*)(qp + 192);
      sc = mfma(kv[0], qa, f4{0.f, 0.f, 0.f, 0.f});
      sc2 = mfma(kv[1], qb, f4{0.f, 0.f, 0.f, 0.f});
      sc = mfma(kv[2], qc, sc);
      sc2 = mfma(kv[3], qd, sc2);
      sc += sc2;
    }
    {
      const unsigned a = pk2(sc[0], sc[1]), bq = pk2(sc[2], sc[3]);
      const unsigned a2 = pk2(bflo(a) * 0.08838834764831845f, bfhi(a) * 0.08838834764831845f);
      const unsigned b2 = pk2(bflo(bq) * 0.08838834764831845f, bfhi(bq) * 0.08838834764831845f);
      sc = f4{bflo(a2), bfhi(a2), bflo(b2), bfhi(b2)};
    }
    float mw = fmaxf(fmaxf(sc[0], sc[1]), fmaxf(sc[2], sc[3]));
    mw = x32_max(x16_max(mw));
    if (lane < 8) *(LAS float*)(L + L_MX + (lane * 8 + w) * 4) = mw;
    STAMP(13);
    BAR();
    float m;
    {
      const f4 a = *(LAS f4*)(L + L_MX + (c & 7) * 32), bq = *(LAS f4*)(L + L_MX + (c & 7) * 32 + 16);
      m = fmaxf(fmaxf(fmaxf(a[0], a[1]), fmaxf(a[2], a[3])), fmaxf(fmaxf(bq[0], bq[1]), fmaxf(bq[2], bq[3])));
    }
    float p[4];
#pragma unroll
    for (int r = 0; r < 4; ++r) p[r] = __builtin_amdgcn_exp2f((sc[r] - m) * LOG2E);
    if (c < 8) *(LAS v2u*)(L + L_P + c * 272 + (16 * w + 4 * gp) * 2) = v2u{pk2(p[0], p[1]), pk2(p[2], p[3])};
    float lw = (p[0] + p[1]) + (p[2] + p[3]);
    lw = x32_sum(x16_sum(lw));
    if (lane < 8) *(LAS float*)(L + L_LS + (lane * 8 + w) * 4) = lw;
    STAMP(14);
    BAR();
    f4 ov, ov2;
    {
      v4u va[4], pa[4];
#pragma unroll
      for (int t = 0; t < 4; ++t) {
        const int row = 32 * t + 8 * gp + (c >> 2), ch = 2 * w + ((c & 3) >> 1);
        const s4 a0 = __builtin_amdgcn_ds_read_tr16_b64_v4i16((LAS s4*)(L + L_V + voff_b(row, ch) + 8 * (c & 1)));
        const s4 a1 = __builtin_amdgcn_ds_read_tr16_b64_v4i16((LAS s4*)(L + L_V + voff_b(row + 4, ch) + 8 * (c & 1)));
        const v2u u0 = __builtin_bit_cast(v2u, a0), u1 = __builtin_bit_cast(v2u, a1);
        va[t] = v4u{u0[0], u0[1], u1[0], u1[1]};
        pa[t] = *(LAS v4u*)(L + L_P + (c & 7) * 272 + t * 64 + gp * 16);
      }
      ov = mfma(va[0], pa[0], f4{0.f, 0.f, 0.f, 0.f});
      ov2 = mfma(va[1], pa[1], f4{0.f, 0.f, 0.f, 0.f});
      ov = mfma(va[2], pa[2], ov);
      ov2 = mfma(va[3], pa[3], ov2);
      ov += ov2;
    }
    if (c < 8) stx(rW, WS_X2O + ((((g * 8 + c) * 8 + w) * 32 + j) * 16 + 4 * gp) * 4, tag4(ov, T));
    if (w == 0 && lane < 8) {
      const f4 a = *(LAS f4*)(L + L_LS + lane * 32), bq = *(LAS f4*)(L + L_LS + lane * 32 + 16);
      const float l = ((a[0] + a[1]) + (a[2] + a[3])) + ((bq[0] + bq[1]) + (bq[2] + bq[3]));
      stx(rW, WS_X2M + ((g * 8 + lane) * 32 + j) * 16, tag4(f4{m, l, 0.f, 0.f}, T));
    }
    STAMP(6);
    sfor<17 + WA2, 25>(issue);
    SB();
    if constexpr (X2_WAIT > 0) TOUCH(wl[X2_WAIT - 1]);
    SB();
    STAMP(17);
    {
      const int oo = WS_X2O + (slot * 8 + w) * 2048 + lane * 16;
      const int mo = WS_X2M + (slot * 32 + (lane >> 2) + 16 * (lane & 1)) * 16;
      v4u ml = ldx(rW, mo), o0 = ldx(rW, oo), o1 = ldx(rW, oo + 1024);
      unsigned bad = bad4(o0, T) | bad4(o1, T) | bad4(ml, T);
      int spins = 0;
      if (__any(bad != 0)) {
        do {
          CFENCE();
          __builtin_amdgcn_s_sleep(POLL_SLEEP);
          if (bad != 0) { ml = ldx(rW, mo); o0 = ldx(rW, oo); o1 = ldx(rW, oo + 1024); bad = bad4(o0, T) | bad4(o1, T) | bad4(ml, T); }
        } while (__any(bad != 0) & (++spins < SPIN_CAP));
        ++spins;
      }
      STAMPV(21, spins);
      STAMP(7);
      const float ms = asf(ml[0] & ~TAGM), ls = asf(ml[1] & ~TAGM);
      float M = fmaxf(ms, dppf<XOR1>(ms));
      M = fmaxf(M, dppf<ROR4>(M)); M = fmaxf(M, dppf<ROR8>(M));
      M = x32_max(x16_max(M));
      const float e = __builtin_amdgcn_exp2f((ms - M) * LOG2E);
      float Lt = e * ls;
      Lt += dppf<XOR1>(Lt); Lt += dppf<ROR4>(Lt); Lt += dppf<ROR8>(Lt);
      Lt = x32_sum(x16_sum(Lt));
      const f4 z = val4(o0) * dppf<QB0>(e) + val4(o1) * dppf<QB1>(e);
      float o = swap16_add(swap32_add(z[0], z[1]), swap32_add(z[2], z[3]));
      o += dppf<ROR4>(o); o += dppf<ROR8>(o);
      o = o / Lt;
      xl = pk2(o, o);
    }
  } else {
    const int idx = xcd * 14 + (li - 16), G = idx >> 2, j = idx & 3;
    const int kh = j & 1;
    const unsigned own = __builtin_amdgcn_raw_buffer_load_b16(rX, (512 * kh + 64 * w + lane) * 2, 0, 0);
    cq = j; slot = 32 + G;
    v4u gu[16];
    const rsrc_t rg = mkrs((j >> 1) ? wu : wg, D * DF * 2), rd = mkrs(wd, DF * D * 2);
    const int vog = (512 * kh + 64 * w + 16 * gp) * 7168 + (128 * G + 8 * c) * 2;
    int vod;
    auto issue = [&](auto I) {
      constexpr int k = decltype(I)::v;
      if constexpr (k < 16) gu[k] = ldw(rg, vog, k * 7168);
      else wl[k - 16] = ldw(rd, vod + ((k - 16) & 1) * 256, ((k - 16) >> 1) * 8192);
    };
    sfor<0, WB0>([&](auto I) { SB(); BAR0(); issue(I); });
    SB();
    vod = (128 * G + 16 * w + kg) * 2048 + (256 * cq + 8 * c) * 2;
    TOUCH(xa); TOUCH(xb);
    const unsigned xn = ln_own(xa, xb, own);
    setup();
    SB();
    STAMP(1);
    f4 acc = {0.f, 0.f, 0.f, 0.f};
    sfor<0, 16>([&](auto I) {
      constexpr int t = decltype(I)::v;
      acc = mfma(aop(dppu<NBC + t>(xn), mk), gu[t], acc);
      SB();
      sfor<upB(t - 1), upB(t)>(issue);
      SB();
    });
    sfor<upB(15), 16 + WB2>(issue);
    SB();
    if (lane < 32) *(LAS f4*)(L + L_RED + w * 512 + (8 * c + 4 * gp) * 4) = acc;
    STAMP(2);
    BAR();
    if (w == 0) {
      const int hf = lane >> 5, ch = lane & 31;
      f4 s = *(LAS f4*)(L + L_RED + (4 * hf) * 512 + ch * 16);
#pragma unroll
      for (int i = 1; i < 4; ++i) s += *(LAS f4*)(L + L_RED + (4 * hf + i) * 512 + ch * 16);
      s[0] = x32_sum(s[0]); s[1] = x32_sum(s[1]); s[2] = x32_sum(s[2]); s[3] = x32_sum(s[3]);
      if (lane < 32) stx(rW, WS_X3 + (G * 4 + j) * 512 + ch * 16, tag4(s, T));
    }
    SB();
    STAMP(3);
    sfor<16 + WB2, 24>([&](auto I) { constexpr int k = decltype(I)::v; TOUCH(wl[k - 16 - WB2]); SB(); issue(I); SB(); });
    if constexpr (X3_WAIT > 0) TOUCH(wl[X3_WAIT - 1]);
    SB();
    STAMP(4);
    const int ho = WS_X3 + (G * 4 + ((c >> 1) & 2) + (c >> 3)) * 512 + (4 * w + (c & 3)) * 16;
    v4u hv;
    int spins = 0;
    do {
      CFENCE();
      hv = ldx(rW, ho);
    } while (__any(bad4(hv, T) != 0) & (++spins < SPIN_CAP));
    STAMPV(21, spins);
    STAMP(7);
    {
      const unsigned e01 = (gp & 2) ? hv[1] : hv[0], e23 = (gp & 2) ? hv[3] : hv[2];
      const float pv = asf(((gp & 1) ? e23 : e01) & ~TAGM);
      const float s = pv + dppf<ROR8>(pv);
      const float o = dppf<ROR4>(s);
      const float gg = rbf((c & 4) ? o : s), uu = rbf((c & 4) ? s : o);
      const float y = gg * fmaf(gg * gg, 0.044715f * 0.7978845608028654f * -2.f * LOG2E, 0.7978845608028654f * -2.f * LOG2E);
      const float ge = gg * __builtin_amdgcn_rcpf(1.f + __builtin_amdgcn_exp2f(y));
      const float hh = rbf(ge) * uu;
      xl = pk2(hh, hh);
    }
  }

  {
    f4 a0 = {0.f, 0.f, 0.f, 0.f}, a1 = {0.f, 0.f, 0.f, 0.f};
    sfor<0, 4>([&](auto I) {
      constexpr int t = decltype(I)::v;
      const v4u a = aop(dppu<NBC + t>(xl), mk);
      a0 = mfma(a, wl[2 * t], a0);
      a1 = mfma(a, wl[2 * t + 1], a1);
    });
    if (lane < 32) {
      *(LAS f4*)(L + L_RED3 + w * 1024 + (8 * c + 4 * gp) * 4) = a0;
      *(LAS f4*)(L + L_RED3 + w * 1024 + 512 + (8 * c + 4 * gp) * 4) = a1;
    }
    STAMP(8);
    BAR();
    if (w == 0) {
      f4 s = *(LAS f4*)(L + L_RED3 + lane * 16);
#pragma unroll
      for (int i = 1; i < 8; ++i) s += *(LAS f4*)(L + L_RED3 + i * 1024 + lane * 16);
      stx(rW, WS_X4 + (cq * 64 + slot) * 1024 + lane * 16, tag4(s, T));
      STAMP(9);
    }
  }
  const int rid = RED_ROLE == 0 ? (li < 8 ? li * 8 + xcd : 64) : (li >= 16 && li < 24 ? (li - 16) * 8 + xcd : 64);
  if (w == 1 && rid < 64) {
    const int off = WS_X4 + ((rid >> 4) * 64 + (lane >> 2)) * 1024 + (rid & 15) * 64 + (lane & 3) * 16;
    const int off3 = lane < 48 ? off + 48 * 1024 : off;
    const int col = rid * 16 + 4 * (c & 3) + ((c >> 2) & 2) + (gp & 1);
    const unsigned xo = __builtin_amdgcn_raw_buffer_load_b16(rX, col * 2, 0, 0);
    if (X4_DELAY > 0) __builtin_amdgcn_s_sleep(X4_DELAY);
    v4u v0 = ldx(rW, off), v1 = ldx(rW, off + 16 * 1024), v2 = ldx(rW, off + 32 * 1024), v3 = ldx(rW, off3);
    unsigned b0 = bad4(v0, T), b1 = bad4(v1, T), b2 = bad4(v2, T), b3 = bad4(v3, T);
    int spins = 0;
    while (__any((b0 | b1 | b2 | b3) != 0) & (++spins < SPIN_CAP)) {
      CFENCE();
      if (b0 != 0) v0 = ldx(rW, off);
      if (b1 != 0) v1 = ldx(rW, off + 16 * 1024);
      if (b2 != 0) v2 = ldx(rW, off + 32 * 1024);
      if (b3 != 0) v3 = ldx(rW, off3);
      b0 = bad4(v0, T); b1 = bad4(v1, T); b2 = bad4(v2, T); b3 = bad4(v3, T);
    }
    STAMPV(22, spins);
    STAMP(10);
    const f4 ya = val4(v0) + val4(v1);
    f4 yb = val4(v3);
    if (lane >= 48) yb = f4{0.f, 0.f, 0.f, 0.f};
    yb += val4(v2);
    const float z0 = swap32_add(ya[0], yb[0]), z1 = swap32_add(ya[1], yb[1]), z2 = swap32_add(ya[2], yb[2]), z3 = swap32_add(ya[3], yb[3]);
    float y0 = swap16_add(z0, z1), y1 = swap16_add(z2, z3);
    y0 += dppf<ROR4>(y0); y1 += dppf<ROR4>(y1);
    y0 += dppf<ROR8>(y0); y1 += dppf<ROR8>(y1);
    const float z = (c & 8) ? y1 : y0;
    auto sw = __builtin_amdgcn_permlane32_swap(asu(z), asu(z), false, false);
    const float res = rbf(asf(xo << 16) + rbf(asf(sw[0]))) + rbf(asf(sw[1]));
    if (lane < 32 && (c & 4) == 0) out[col] = (u16)(pk2(res, res) & 0xffffu);
    if (rid == 0 && lane == 0) __builtin_amdgcn_raw_buffer_store_b32(T, rW, WS_E, 0, AUX_SC1);
    STAMP(11);
  }
  STAMP_OUT();
}

extern "C" void kernel_launch(void* const* d_in, const int* in_sizes, int n_in, void* d_out, int out_size, void* d_ws, size_t ws_size,
                              hipStream_t stream) {
  fused_k<<<NWG, NTHR, 0, stream>>>((const u16*)d_in[0], (const u16*)d_in[1], (const u16*)d_in[2], (const u16*)d_in[3], (const u16*)d_in[4],
                                    (const u16*)d_in[6], (const u16*)d_in[7], (const u16*)d_in[8], (u16*)d_out, (unsigned char*)d_ws);
}
```

```cpp
#define AMP3 0
#include <hip/hip_runtime.h>
#include <hip/hip_bf16.h>
#include <stdint.h>

typedef unsigned short u16;
typedef unsigned long long u64;
typedef unsigned v4u __attribute__((ext_vector_type(4)));
typedef unsigned v2u __attribute__((ext_vector_type(2)));
typedef float f4 __attribute__((ext_vector_type(4)));
typedef float f2 __attribute__((ext_vector_type(2)));
typedef __bf16 bf8 __attribute__((ext_vector_type(8)));
typedef __bf16 bf2 __attribute__((ext_vector_type(2)));
typedef short s4 __attribute__((ext_vector_type(4)));
#define LAS __attribute__((address_space(3)))
#define DEV __device__ __forceinline__

#define W_AUX 2
#define WA0 6
#define WA1 5
#define WAK 4
#define WA2 6
#define WB0 2
#define NB0 6
#define WB1 4
#define WB2 5
#define X2_WAIT 7
#define X3_WAIT 4
#define X4_DELAY 4
#define POLL_SLEEP 2
#define RED_ROLE 1

constexpr int D = 1024, HQ = 32, HKV = 4, SK = 32768, DH = 128, WIN = 4096, DF = 3584;
constexpr int NWG = 240, NTHR = 512;
constexpr int AUX_SC1 = 16;
constexpr unsigned TAGM = 0x3FFu;
constexpr int SPIN_CAP = 1 << 16;
constexpr float LOG2E = 1.4426950408889634f;

constexpr int WS_E = 0;
constexpr int WS_X1 = 4096;
constexpr int WS_X2O = WS_X1 + 65536;
constexpr int WS_X2M = WS_X2O + 524288;
constexpr int WS_X3 = WS_X2M + 16384;
constexpr int WS_X4 = WS_X3 + 65536;
constexpr int WS_END = WS_X4 + 262144;

constexpr int L_V = 0;
constexpr int L_K = 32768;
constexpr int L_RED = 65536;
constexpr int L_Q = L_RED + 4096;
constexpr int L_P = L_Q + 8 * 272;
constexpr int L_MX = L_P + 8 * 272;
constexpr int L_LS = L_MX + 256;
constexpr int L_RED3 = L_LS + 256;
constexpr int L_END = L_RED3 + 8192;
constexpr int LDS_BYTES = 84 * 1024;
static_assert(L_END <= LDS_BYTES, "lds");

#define STAMP(i) do {} while (0)
#define STAMPV(i, v) do {} while (0)
#define STAMP_DECL
#define STAMP_OUT() do { if (w == 1 && rid == 0) { \
   u64 now_; const u64 until_ = tA + 2500 + (AMP3) * (tB - tA); do { __builtin_amdgcn_s_sleep(1); asm volatile("s_memrealtime %0\n\ts_waitcnt lgkmcnt(0)" : "=s"(now_) :: "memory"); } while (now_ < until_); } } while (0)

DEV float asf(unsigned u) { return __builtin_bit_cast(float, u); }
DEV unsigned asu(float f) { return __builtin_bit_cast(unsigned, f); }
DEV float bflo(unsigned w) { return asf(w << 16); }
DEV float bfhi(unsigned w) { return asf(w & 0xffff0000u); }
DEV unsigned pk2(float lo, float hi) { f2 v = {lo, hi}; return __builtin_bit_cast(unsigned, __builtin_convertvector(v, bf2)); }
DEV float rbf(float f) { return asf(pk2(f, f) & 0xffff0000u); }
template <int C> DEV float dppf(float x) { return asf(__builtin_amdgcn_mov_dpp(asu(x), C, 0xf, 0xf, true)); }
template <int C> DEV unsigned dppu(unsigned x) { return __builtin_amdgcn_mov_dpp(x, C, 0xf, 0xf, true); }
constexpr int XOR1 = 0xB1, XOR2 = 0x4E, HMIR = 0x141, ROR4 = 0x124, ROR8 = 0x128, NBC = 0x150, QB0 = 0x00, QB1 = 0x55;
DEV float swap16_add(float a, float b) { auto s = __builtin_amdgcn_permlane16_swap(asu(a), asu(b), false, false); return asf(s[0]) + asf(s[1]); }
DEV float swap32_add(float a, float b) { auto s = __builtin_amdgcn_permlane32_swap(asu(a), asu(b), false, false); return asf(s[0]) + asf(s[1]); }
DEV float x16_sum(float x) { return swap16_add(x, x); }
DEV float x32_sum(float x) { return swap32_add(x, x); }
DEV float x16_max(float x) { auto s = __builtin_amdgcn_permlane16_swap(asu(x), asu(x), false, false); return fmaxf(asf(s[0]), asf(s[1])); }
DEV float x32_max(float x) { auto s = __builtin_amdgcn_permlane32_swap(asu(x), asu(x), false, false); return fmaxf(asf(s[0]), asf(s[1])); }
DEV float row_sum(float x) { x += dppf<XOR1>(x); x += dppf<XOR2>(x); x += dppf<HMIR>(x); x += dppf<ROR8>(x); return x; }
DEV float wave_sum(float x) { return x32_sum(x16_sum(row_sum(x))); }
DEV f4 mfma(v4u a, v4u b, f4 c) { return __builtin_amdgcn_mfma_f32_16x16x32_bf16(__builtin_bit_cast(bf8, a), __builtin_bit_cast(bf8, b), c, 0, 0, 0); }
#define SB() __builtin_amdgcn_sched_barrier(0)
#define BAR() asm volatile("s_waitcnt lgkmcnt(0)\n\ts_barrier" ::: "memory")
#define BAR0() asm volatile("s_barrier" ::: "memory")
#define CFENCE() asm volatile("" ::: "memory")
#define TOUCH(x) asm volatile("" : "+v"(x))

typedef __amdgpu_buffer_rsrc_t rsrc_t;
DEV rsrc_t mkrs(const void* p, unsigned bytes) { return __builtin_amdgcn_make_buffer_rsrc((void*)p, 0, bytes, 0x00020000); }
DEV v4u ldw(rsrc_t r, int voff, int soff) { return __builtin_amdgcn_raw_buffer_load_b128(r, voff, soff, W_AUX); }
DEV v4u ldx(rsrc_t r, int voff) { return __builtin_amdgcn_raw_buffer_load_b128(r, voff, 0, AUX_SC1); }
DEV void stx(rsrc_t r, int voff, v4u v) { __builtin_amdgcn_raw_buffer_store_b128(v, r, voff, 0, AUX_SC1); }
DEV unsigned tag1(float f, unsigned T) { return ((asu(f) + 0x200u) & ~TAGM) | T; }
DEV v4u tag4(f4 v, unsigned T) { return v4u{tag1(v[0], T), tag1(v[1], T), tag1(v[2], T), tag1(v[3], T)}; }
DEV unsigned bad4(v4u v, unsigned T) { return ((v[0] ^ T) | (v[1] ^ T) | (v[2] ^ T) | (v[3] ^ T)) & TAGM; }
DEV f4 val4(v4u v) { return f4{asf(v[0] & ~TAGM), asf(v[1] & ~TAGM), asf(v[2] & ~TAGM), asf(v[3] & ~TAGM)}; }

template <int I> struct IC { static constexpr int v = I; };
template <int LO, int HI, class F> DEV void sfor(F&& f) { if constexpr (LO < HI) { f(IC<LO>{}); sfor<LO + 1, HI>(f); } }
constexpr int imin(int a, int b) { return a < b ? a : b; }
constexpr int imax(int a, int b) { return a > b ? a : b; }
constexpr int upA(int t) { return t < 0 ? WA0 : imin(17 + WA2, imax(upA(t - 1), t + 1 + (t < 7 ? WA1 : WAK))); }
constexpr int upB(int t) { return t < 0 ? WB0 : imin(16 + WB2, imax(upB(t - 1), t + 1 + (t + 1 < NB0 ? WB0 : WB1))); }

struct Masks { unsigned m[4]; };
DEV v4u aop(unsigned xr, const Masks& k) { return v4u{xr & k.m[0], xr & k.m[1], xr & k.m[2], xr & k.m[3]}; }

DEV unsigned ln_own(v4u xa, v4u xb, unsigned own) {
  float v[16];
#pragma unroll
  for (int i = 0; i < 4; ++i) { v[2 * i] = bflo(xa[i]); v[2 * i + 1] = bfhi(xa[i]); v[8 + 2 * i] = bflo(xb[i]); v[9 + 2 * i] = bfhi(xb[i]); }
  float s = 0.f;
#pragma unroll
  for (int i = 0; i < 16; ++i) s += v[i];
  const float mean = wave_sum(s) * (1.f / D);
  float q = 0.f;
#pragma unroll
  for (int i = 0; i < 16; ++i) { const float d = v[i] - mean; q = fmaf(d, d, q); }
  const float var = wave_sum(q) * (1.f / D);
  const float den = rbf(__builtin_sqrtf(rbf(rbf(var) + 1e-5f)));
  const float xn = rbf(asf(own << 16) - rbf(mean)) / den;
  return pk2(xn, xn);
}

DEV unsigned voff_b(unsigned row, unsigned ch) { return 256u * row + 16u * (ch ^ (((row & 3) << 2) | ((row >> 2) & 3))); }

__global__ void __launch_bounds__(NTHR, 2)
fused_k(const u16* x_, const u16* kc_, const u16* vc_, const u16* wq_, const u16* wo_, const u16* wg_, const u16* wu_, const u16* wd_,
        u16* out_, unsigned char* ws_
) {
  __shared__ __attribute__((aligned(16))) unsigned char lds_raw[LDS_BYTES];
  typedef unsigned long long u64x8 __attribute__((ext_vector_type(8)));
  typedef unsigned long long u64x2 __attribute__((ext_vector_type(2)));
  u64 tA, tB; u64x8 ka; u64x2 kb;
  asm volatile("s_memrealtime %0\n\ts_waitcnt lgkmcnt(0)" : "=s"(tA) :: "memory");
  asm volatile("s_load_dwordx16 %0, %2, 0x0\n\ts_load_dwordx4 %1, %2, 0x40\n\ts_waitcnt lgkmcnt(0)" : "=&s"(ka), "=&s"(kb) : "s"(__builtin_amdgcn_kernarg_segment_ptr()) : "memory");
  asm volatile("s_memrealtime %0\n\ts_waitcnt lgkmcnt(0)" : "=s"(tB) :: "memory");
  const u16* x = (const u16*)ka[0]; const u16* kc = (const u16*)ka[1]; const u16* vc = (const u16*)ka[2]; const u16* wq = (const u16*)ka[3];
  const u16* wo = (const u16*)ka[4]; const u16* wg = (const u16*)ka[5]; const u16* wu = (const u16*)ka[6]; const u16* wd = (const u16*)ka[7];
  __attribute__((address_space(1))) u16* out = (__attribute__((address_space(1))) u16*)kb[0]; unsigned char* ws = (unsigned char*)kb[1];
  LAS unsigned char* L = (LAS unsigned char*)lds_raw;
  const int tid = threadIdx.x, lane = tid & 63, w = __builtin_amdgcn_readfirstlane(tid >> 6);
  const int gp = lane >> 4, c = lane & 15;
  const int kg = 2 * (gp & 1) + (gp >> 1);
  const int b = blockIdx.x, xcd = b & 7, li = b >> 3;
  STAMP_DECL
  STAMP(0);
  const rsrc_t rW = mkrs(ws, WS_END);
  const rsrc_t rX = mkrs(x, D * 2);
  v4u xa = __builtin_amdgcn_raw_buffer_load_b128(rX, lane * 16, 0, 0);
  v4u xb = __builtin_amdgcn_raw_buffer_load_b128(rX, lane * 16 + 1024, 0, 0);
  const unsigned e0 = __builtin_amdgcn_raw_buffer_load_b32(rW, WS_E, 0, 0);
  unsigned T;
  Masks mk;
  auto setup = [&]() {
    T = (__builtin_amdgcn_readfirstlane(e0) + 1u) & TAGM;
    int cc = c; TOUCH(cc);
#pragma unroll
    for (int d = 0; d < 4; ++d) mk.m[d] = (cc == 2 * d) ? 0x0000ffffu : (cc == 2 * d + 1) ? 0xffff0000u : 0u;
  };
  unsigned xl;
  v4u wl[8];
  int slot, cq;

  if (li < 16) {
    const int g = xcd >> 1, j = (xcd & 1) * 16 + li;
    const int ct = j & 7, ks = j >> 3;
    const unsigned own = __builtin_amdgcn_raw_buffer_load_b16(rX, (256 * ks + 32 * w + 8 * gp + (c & 7)) * 2, 0, 0);
    cq = j & 3; slot = 8 * g + (j >> 2);
    v4u wqv[8], kv[4], vv[4], q0, q1;
    const rsrc_t rq = mkrs(wq, D * HQ * DH * 2), rk = mkrs(kc, HKV * SK * DH * 2), rv = mkrs(vc, HKV * SK * DH * 2), ro = mkrs(wo, HQ * DH * D * 2);
    const int voq = (256 * ks + 32 * w + 8 * gp) * 8192 + ((8 * g + ct) * 128 + 8 * c) * 2;
    int vokv, voo, x1off;
    auto issue = [&](auto I) {
      constexpr int k = decltype(I)::v;
      if constexpr (k < 8) wqv[k] = ldw(rq, voq, k * 8192);
      else if constexpr (k < 12) kv[k - 8] = ldw(rk, vokv + (k - 8) * 1024, 0);
      else if constexpr (k < 16) vv[k - 12] = ldw(rv, vokv + (k - 12) * 1024, 0);
      else if constexpr (k == 16) { q0 = ldx(rW, x1off); q1 = ldx(rW, x1off + 4096); }
      else wl[k - 17] = ldw(ro, voo + ((k - 17) & 1) * 256, ((k - 17) >> 1) * 8192);
    };
    sfor<0, WA0>([&](auto I) { SB(); BAR0(); issue(I); });
    SB();
    vokv = ((g * SK + (SK - WIN) + 128 * j + 16 * w) * DH) * 2 + lane * 16;
    voo = (128 * slot + 16 * w + kg) * 2048 + (256 * cq + 8 * c) * 2;
    x1off = WS_X1 + ((g * 4 + 2 * (lane >> 5)) * 1024 + w * 128) * 4 + (lane & 31) * 16;
    TOUCH(xa); TOUCH(xb);
    const unsigned xn = ln_own(xa, xb, own);
    setup();
    SB();
    STAMP(1);
    f4 acc = {0.f, 0.f, 0.f, 0.f};
    sfor<0, 8>([&](auto I) {
      constexpr int t = decltype(I)::v;
      acc = mfma(aop(dppu<NBC + t>(xn), mk), wqv[t], acc);
      SB();
      sfor<upA(t - 1), upA(t)>(issue);
      SB();
    });
    if (lane < 32) *(LAS f4*)(L + L_RED + w * 512 + (8 * c + 4 * gp) * 4) = acc;
    STAMP(2);
    BAR();
    if (w == 0) {
      const int hf = lane >> 5, ch = lane & 31;
      f4 s = *(LAS f4*)(L + L_RED + (4 * hf) * 512 + ch * 16);
#pragma unroll
      for (int i = 1; i < 4; ++i) s += *(LAS f4*)(L + L_RED + (4 * hf + i) * 512 + ch * 16);
      s[0] = x32_sum(s[0]); s[1] = x32_sum(s[1]); s[2] = x32_sum(s[2]); s[3] = x32_sum(s[3]);
      if (lane < 32) stx(rW, WS_X1 + ((g * 4 + ks) * 1024 + ct * 128) * 4 + ch * 16, tag4(s, T));
    }
    SB();
    STAMP(3);
    sfor<8, 16>([&](auto I) {
      constexpr int t = decltype(I)::v;
      if constexpr (t < 12) *(LAS v4u*)(L + L_K + voff_b(16 * w + 4 * (t - 8) + gp, c)) = kv[t - 8];
      else *(LAS v4u*)(L + L_V + voff_b(16 * w + 4 * (t - 12) + gp, c)) = vv[t - 12];
      SB();
      sfor<upA(t - 1), upA(t)>(issue);
      if constexpr (t == 11) {
#pragma unroll
        for (int i = 0; i < 4; ++i) kv[i] = *(LAS v4u*)(L + L_K + voff_b(16 * w + c, 4 * i + gp));
      }
      SB();
    });
    sfor<upA(15), 17 + WA2>(issue);
    SB();
    STAMP(4);
    {
      int spins = 0;
      unsigned bad = bad4(q0, T) | bad4(q1, T);
      if (__builtin_expect(__any(bad != 0), 0)) {
        do {
          CFENCE();
          if (bad != 0) { q0 = ldx(rW, x1off); q1 = ldx(rW, x1off + 4096); bad = bad4(q0, T) | bad4(q1, T); }
        } while (__any(bad != 0) & (++spins < SPIN_CAP));
        ++spins;
      }
      STAMPV(20, spins);
    }
    STAMP(5);
    {
      const f4 s = val4(q0) + val4(q1);
      const float a = swap32_add(s[0], s[2]), bq = swap32_add(s[1], s[3]);
      *(LAS unsigned*)(L + L_Q + w * 272 + (lane & 31) * 8 + (lane >> 5) * 4) = pk2(a, bq);
    }
    BAR();
    STAMP(12);
    f4 sc, sc2;
    {
      const LAS unsigned char* qp = L + L_Q + (c & 7) * 272 + gp * 16;
      const v4u qa = *(LAS v4u*)(qp), qb = *(LAS v4u*)(qp + 64), qc = *(LAS v4u*)(qp + 128), qd = *(LAS v4u*)(qp + 192);
      sc = mfma(kv[0], qa, f4{0.f, 0.f, 0.f, 0.f});
      sc2 = mfma(kv[1], qb, f4{0.f, 0.f, 0.f, 0.f});
      sc = mfma(kv[2], qc, sc);
      sc2 = mfma(kv[3], qd, sc2);
      sc += sc2;
    }
    {
      const unsigned a = pk2(sc[0], sc[1]), bq = pk2(sc[2], sc[3]);
      const unsigned a2 = pk2(bflo(a) * 0.08838834764831845f, bfhi(a) * 0.08838834764831845f);
      const unsigned b2 = pk2(bflo(bq) * 0.08838834764831845f, bfhi(bq) * 0.08838834764831845f);
      sc = f4{bflo(a2), bfhi(a2), bflo(b2), bfhi(b2)};
    }
    float mw = fmaxf(fmaxf(sc[0], sc[1]), fmaxf(sc[2], sc[3]));
    mw = x32_max(x16_max(mw));
    if (lane < 8) *(LAS float*)(L + L_MX + (lane * 8 + w) * 4) = mw;
    STAMP(13);
    BAR();
    float m;
    {
      const f4 a = *(LAS f4*)(L + L_MX + (c & 7) * 32), bq = *(LAS f4*)(L + L_MX + (c & 7) * 32 + 16);
      m = fmaxf(fmaxf(fmaxf(a[0], a[1]), fmaxf(a[2], a[3])), fmaxf(fmaxf(bq[0], bq[1]), fmaxf(bq[2], bq[3])));
    }
    float p[4];
#pragma unroll
    for (int r = 0; r < 4; ++r) p[r] = __builtin_amdgcn_exp2f((sc[r] - m) * LOG2E);
    if (c < 8) *(LAS v2u*)(L + L_P + c * 272 + (16 * w + 4 * gp) * 2) = v2u{pk2(p[0], p[1]), pk2(p[2], p[3])};
    float lw = (p[0] + p[1]) + (p[2] + p[3]);
    lw = x32_sum(x16_sum(lw));
    if (lane < 8) *(LAS float*)(L + L_LS + (lane * 8 + w) * 4) = lw;
    STAMP(14);
    BAR();
    f4 ov, ov2;
    {
      v4u va[4], pa[4];
#pragma unroll
      for (int t = 0; t < 4; ++t) {
        const int row = 32 * t + 8 * gp + (c >> 2), ch = 2 * w + ((c & 3) >> 1);
        const s4 a0 = __builtin_amdgcn_ds_read_tr16_b64_v4i16((LAS s4*)(L + L_V + voff_b(row, ch) + 8 * (c & 1)));
        const s4 a1 = __builtin_amdgcn_ds_read_tr16_b64_v4i16((LAS s4*)(L + L_V + voff_b(row + 4, ch) + 8 * (c & 1)));
        const v2u u0 = __builtin_bit_cast(v2u, a0), u1 = __builtin_bit_cast(v2u, a1);
        va[t] = v4u{u0[0], u0[1], u1[0], u1[1]};
        pa[t] = *(LAS v4u*)(L + L_P + (c & 7) * 272 + t * 64 + gp * 16);
      }
      ov = mfma(va[0], pa[0], f4{0.f, 0.f, 0.f, 0.f});
      ov2 = mfma(va[1], pa[1], f4{0.f, 0.f, 0.f, 0.f});
      ov = mfma(va[2], pa[2], ov);
      ov2 = mfma(va[3], pa[3], ov2);
      ov += ov2;
    }
    if (c < 8) stx(rW, WS_X2O + ((((g * 8 + c) * 8 + w) * 32 + j) * 16 + 4 * gp) * 4, tag4(ov, T));
    if (w == 0 && lane < 8) {
      const f4 a = *(LAS f4*)(L + L_LS + lane * 32), bq = *(LAS f4*)(L + L_LS + lane * 32 + 16);
      const float l = ((a[0] + a[1]) + (a[2] + a[3])) + ((bq[0] + bq[1]) + (bq[2] + bq[3]));
      stx(rW, WS_X2M + ((g * 8 + lane) * 32 + j) * 16, tag4(f4{m, l, 0.f, 0.f}, T));
    }
    STAMP(6);
    sfor<17 + WA2, 25>(issue);
    SB();
    if constexpr (X2_WAIT > 0) TOUCH(wl[X2_WAIT - 1]);
    SB();
    STAMP(17);
    {
      const int oo = WS_X2O + (slot * 8 + w) * 2048 + lane * 16;
      const int mo = WS_X2M + (slot * 32 + (lane >> 2) + 16 * (lane & 1)) * 16;
      v4u ml = ldx(rW, mo), o0 = ldx(rW, oo), o1 = ldx(rW, oo + 1024);
      unsigned bad = bad4(o0, T) | bad4(o1, T) | bad4(ml, T);
      int spins = 0;
      if (__any(bad != 0)) {
        do {
          CFENCE();
          __builtin_amdgcn_s_sleep(POLL_SLEEP);
          if (bad != 0) { ml = ldx(rW, mo); o0 = ldx(rW, oo); o1 = ldx(rW, oo + 1024); bad = bad4(o0, T) | bad4(o1, T) | bad4(ml, T); }
        } while (__any(bad != 0) & (++spins < SPIN_CAP));
        ++spins;
      }
      STAMPV(21, spins);
      STAMP(7);
      const float ms = asf(ml[0] & ~TAGM), ls = asf(ml[1] & ~TAGM);
      float M = fmaxf(ms, dppf<XOR1>(ms));
      M = fmaxf(M, dppf<ROR4>(M)); M = fmaxf(M, dppf<ROR8>(M));
      M = x32_max(x16_max(M));
      const float e = __builtin_amdgcn_exp2f((ms - M) * LOG2E);
      float Lt = e * ls;
      Lt += dppf<XOR1>(Lt); Lt += dppf<ROR4>(Lt); Lt += dppf<ROR8>(Lt);
      Lt = x32_sum(x16_sum(Lt));
      const f4 z = val4(o0) * dppf<QB0>(e) + val4(o1) * dppf<QB1>(e);
      float o = swap16_add(swap32_add(z[0], z[1]), swap32_add(z[2], z[3]));
      o += dppf<ROR4>(o); o += dppf<ROR8>(o);
      o = o / Lt;
      xl = pk2(o, o);
    }
  } else {
    const int idx = xcd * 14 + (li - 16), G = idx >> 2, j = idx & 3;
    const int kh = j & 1;
    const unsigned own = __builtin_amdgcn_raw_buffer_load_b16(rX, (512 * kh + 64 * w + lane) * 2, 0, 0);
    cq = j; slot = 32 + G;
    v4u gu[16];
    const rsrc_t rg = mkrs((j >> 1) ? wu : wg, D * DF * 2), rd = mkrs(wd, DF * D * 2);
    const int vog = (512 * kh + 64 * w + 16 * gp) * 7168 + (128 * G + 8 * c) * 2;
    int vod;
    auto issue = [&](auto I) {
      constexpr int k = decltype(I)::v;
      if constexpr (k < 16) gu[k] = ldw(rg, vog, k * 7168);
      else wl[k - 16] = ldw(rd, vod + ((k - 16) & 1) * 256, ((k - 16) >> 1) * 8192);
    };
    sfor<0, WB0>([&](auto I) { SB(); BAR0(); issue(I); });
    SB();
    vod = (128 * G + 16 * w + kg) * 2048 + (256 * cq + 8 * c) * 2;
    TOUCH(xa); TOUCH(xb);
    const unsigned xn = ln_own(xa, xb, own);
    setup();
    SB();
    STAMP(1);
    f4 acc = {0.f, 0.f, 0.f, 0.f};
    sfor<0, 16>([&](auto I) {
      constexpr int t = decltype(I)::v;
      acc = mfma(aop(dppu<NBC + t>(xn), mk), gu[t], acc);
      SB();
      sfor<upB(t - 1), upB(t)>(issue);
      SB();
    });
    sfor<upB(15), 16 + WB2>(issue);
    SB();
    if (lane < 32) *(LAS f4*)(L + L_RED + w * 512 + (8 * c + 4 * gp) * 4) = acc;
    STAMP(2);
    BAR();
    if (w == 0) {
      const int hf = lane >> 5, ch = lane & 31;
      f4 s = *(LAS f4*)(L + L_RED + (4 * hf) * 512 + ch * 16);
#pragma unroll
      for (int i = 1; i < 4; ++i) s += *(LAS f4*)(L + L_RED + (4 * hf + i) * 512 + ch * 16);
      s[0] = x32_sum(s[0]); s[1] = x32_sum(s[1]); s[2] = x32_sum(s[2]); s[3] = x32_sum(s[3]);
      if (lane < 32) stx(rW, WS_X3 + (G * 4 + j) * 512 + ch * 16, tag4(s, T));
    }
    SB();
    STAMP(3);
    sfor<16 + WB2, 24>([&](auto I) { constexpr int k = decltype(I)::v; TOUCH(wl[k - 16 - WB2]); SB(); issue(I); SB(); });
    if constexpr (X3_WAIT > 0) TOUCH(wl[X3_WAIT - 1]);
    SB();
    STAMP(4);
    const int ho = WS_X3 + (G * 4 + ((c >> 1) & 2) + (c >> 3)) * 512 + (4 * w + (c & 3)) * 16;
    v4u hv;
    int spins = 0;
    do {
      CFENCE();
      hv = ldx(rW, ho);
    } while (__any(bad4(hv, T) != 0) & (++spins < SPIN_CAP));
    STAMPV(21, spins);
    STAMP(7);
    {
      const unsigned e01 = (gp & 2) ? hv[1] : hv[0], e23 = (gp & 2) ? hv[3] : hv[2];
      const float pv = asf(((gp & 1) ? e23 : e01) & ~TAGM);
      const float s = pv + dppf<ROR8>(pv);
      const float o = dppf<ROR4>(s);
      const float gg = rbf((c & 4) ? o : s), uu = rbf((c & 4) ? s : o);
      const float y = gg * fmaf(gg * gg, 0.044715f * 0.7978845608028654f * -2.f * LOG2E, 0.7978845608028654f * -2.f * LOG2E);
      const float ge = gg * __builtin_amdgcn_rcpf(1.f + __builtin_amdgcn_exp2f(y));
      const float hh = rbf(ge) * uu;
      xl = pk2(hh, hh);
    }
  }

  {
    f4 a0 = {0.f, 0.f, 0.f, 0.f}, a1 = {0.f, 0.f, 0.f, 0.f};
    sfor<0, 4>([&](auto I) {
      constexpr int t = decltype(I)::v;
      const v4u a = aop(dppu<NBC + t>(xl), mk);
      a0 = mfma(a, wl[2 * t], a0);
      a1 = mfma(a, wl[2 * t + 1], a1);
    });
    if (lane < 32) {
      *(LAS f4*)(L + L_RED3 + w * 1024 + (8 * c + 4 * gp) * 4) = a0;
      *(LAS f4*)(L + L_RED3 + w * 1024 + 512 + (8 * c + 4 * gp) * 4) = a1;
    }
    STAMP(8);
    BAR();
    if (w == 0) {
      f4 s = *(LAS f4*)(L + L_RED3 + lane * 16);
#pragma unroll
      for (int i = 1; i < 8; ++i) s += *(LAS f4*)(L + L_RED3 + i * 1024 + lane * 16);
      stx(rW, WS_X4 + (cq * 64 + slot) * 1024 + lane * 16, tag4(s, T));
      STAMP(9);
    }
  }
  const int rid = RED_ROLE == 0 ? (li < 8 ? li * 8 + xcd : 64) : (li >= 16 && li < 24 ? (li - 16) * 8 + xcd : 64);
  if (w == 1 && rid < 64) {
    const int off = WS_X4 + ((rid >> 4) * 64 + (lane >> 2)) * 1024 + (rid & 15) * 64 + (lane & 3) * 16;
    const int off3 = lane < 48 ? off + 48 * 1024 : off;
    const int col = rid * 16 + 4 * (c & 3) + ((c >> 2) & 2) + (gp & 1);
    const unsigned xo = __builtin_amdgcn_raw_buffer_load_b16(rX, col * 2, 0, 0);
    if (X4_DELAY > 0) __builtin_amdgcn_s_sleep(X4_DELAY);
    v4u v0 = ldx(rW, off), v1 = ldx(rW, off + 16 * 1024), v2 = ldx(rW, off + 32 * 1024), v3 = ldx(rW, off3);
    unsigned b0 = bad4(v0, T), b1 = bad4(v1, T), b2 = bad4(v2, T), b3 = bad4(v3, T);
    int spins = 0;
    while (__any((b0 | b1 | b2 | b3) != 0) & (++spins < SPIN_CAP)) {
      CFENCE();
      if (b0 != 0) v0 = ldx(rW, off);
      if (b1 != 0) v1 = ldx(rW, off + 16 * 1024);
      if (b2 != 0) v2 = ldx(rW, off + 32 * 1024);
      if (b3 != 0) v3 = ldx(rW, off3);
      b0 = bad4(v0, T); b1 = bad4(v1, T); b2 = bad4(v2, T); b3 = bad4(v3, T);
    }
    STAMPV(22, spins);
    STAMP(10);
    const f4 ya = val4(v0) + val4(v1);
    f4 yb = val4(v3);
    if (lane >= 48) yb = f4{0.f, 0.f, 0.f, 0.f};
    yb += val4(v2);
    const float z0 = swap32_add(ya[0], yb[0]), z1 = swap32_add(ya[1], yb[1]), z2 = swap32_add(ya[2], yb[2]), z3 = swap32_add(ya[3], yb[3]);
    float y0 = swap16_add(z0, z1), y1 = swap16_add(z2, z3);
    y0 += dppf<ROR4>(y0); y1 += dppf<ROR4>(y1);
    y0 += dppf<ROR8>(y0); y1 += dppf<ROR8>(y1);
    const float z = (c & 8) ? y1 : y0;
    auto sw = __builtin_amdgcn_permlane32_swap(asu(z), asu(z), false, false);
    const float res = rbf(asf(xo << 16) + rbf(asf(sw[0]))) + rbf(asf(sw[1]));
    if (lane < 32 && (c & 4) == 0) out[col] = (u16)(pk2(res, res) & 0xffffu);
    if (rid == 0 && lane == 0) __builtin_amdgcn_raw_buffer_store_b32(T, rW, WS_E, 0, AUX_SC1);
    STAMP(11);
  }
  STAMP_OUT();
}

extern "C" void kernel_launch(void* const* d_in, const int* in_sizes, int n_in, void* d_out, int out_size, void* d_ws, size_t ws_size,
                              hipStream_t stream) {
  fused_k<<<NWG, NTHR, 0, stream>>>((const u16*)d_in[0], (const u16*)d_in[1], (const u16*)d_in[2], (const u16*)d_in[3], (const u16*)d_in[4],
                                    (const u16*)d_in[6], (const u16*)d_in[7], (const u16*)d_in[8], (u16*)d_out, (unsigned char*)d_ws);
}
```

```cpp
#define AMP 2
#define RED_ROLE 0
#define W_AUX 0
#include <hip/hip_runtime.h>
#include <hip/hip_bf16.h>
#include <stdint.h>

typedef unsigned short u16;
typedef unsigned long long u64;
typedef unsigned v4u __attribute__((ext_vector_type(4)));
typedef unsigned v2u __attribute__((ext_vector_type(2)));
typedef float f4 __attribute__((ext_vector_type(4)));
typedef float f2 __attribute__((ext_vector_type(2)));
typedef __bf16 bf8 __attribute__((ext_vector_type(8)));
typedef __bf16 bf2 __attribute__((ext_vector_type(2)));
typedef short s4 __attribute__((ext_vector_type(4)));
#define LAS __attribute__((address_space(3)))
#define DEV __device__ __forceinline__

#define WA0 6
#define WA1 5
#define WAK 4
#define WA2 6
#define WB0 2
#define NB0 6
#define WB1 4
#define WB2 5
#define X2_WAIT 7
#define X3_WAIT 4
#define X4_DELAY 4
#define POLL_SLEEP 2

constexpr int D = 1024, HQ = 32, HKV = 4, SK = 32768, DH = 128, WIN = 4096, DF = 3584;
constexpr int NWG = 240, NTHR = 512;
constexpr int AUX_SC1 = 16;
constexpr unsigned TAGM = 0x3FFu;
constexpr int SPIN_CAP = 1 << 16;
constexpr float LOG2E = 1.4426950408889634f;

constexpr int WS_E = 0;
constexpr int WS_X1 = 4096;
constexpr int WS_X2O = WS_X1 + 65536;
constexpr int WS_X2M = WS_X2O + 524288;
constexpr int WS_X3 = WS_X2M + 16384;
constexpr int WS_X4 = WS_X3 + 65536;
constexpr int WS_END = WS_X4 + 262144;

constexpr int L_V = 0;
constexpr int L_K = 32768;
constexpr int L_RED = 65536;
constexpr int L_Q = L_RED + 4096;
constexpr int L_P = L_Q + 8 * 272;
constexpr int L_MX = L_P + 8 * 272;
constexpr int L_LS = L_MX + 256;
constexpr int L_RED3 = L_LS + 256;
constexpr int L_END = L_RED3 + 8192;
constexpr int LDS_BYTES = 84 * 1024;
static_assert(L_END <= LDS_BYTES, "lds");

#define STAMP(i) do { if ((i) == 0 || (i) == AMP) asm volatile("s_memrealtime %0\n\ts_waitcnt lgkmcnt(0)" : "=s"(st_[(i) == 0 ? 0 : 1]) :: "memory"); } while (0)
#define STAMPV(i, v) do {} while (0)
#define STAMP_DECL u64 st_[2] = {0,0};
#define STAMP_OUT() do { if (w == 1 && rid == 0) { u64 now_; const u64 until_ = st_[0] + 20 * (st_[1] - st_[0]); do { __builtin_amdgcn_s_sleep(1); asm volatile("s_memrealtime %0\n\ts_waitcnt lgkmcnt(0)" : "=s"(now_) :: "memory"); } while (now_ < until_); } } while (0)

DEV float asf(unsigned u) { return __builtin_bit_cast(float, u); }
DEV unsigned asu(float f) { return __builtin_bit_cast(unsigned, f); }
DEV float bflo(unsigned w) { return asf(w << 16); }
DEV float bfhi(unsigned w) { return asf(w & 0xffff0000u); }
DEV unsigned pk2(float lo, float hi) { f2 v = {lo, hi}; return __builtin_bit_cast(unsigned, __builtin_convertvector(v, bf2)); }
DEV float rbf(float f) { return asf(pk2(f, f) & 0xffff0000u); }
template <int C> DEV float dppf(float x) { return asf(__builtin_amdgcn_mov_dpp(asu(x), C, 0xf, 0xf, true)); }
template <int C> DEV unsigned dppu(unsigned x) { return __builtin_amdgcn_mov_dpp(x, C, 0xf, 0xf, true); }
constexpr int XOR1 = 0xB1, XOR2 = 0x4E, HMIR = 0x141, ROR4 = 0x124, ROR8 = 0x128, NBC = 0x150, QB0 = 0x00, QB1 = 0x55;
DEV float swap16_add(float a, float b) { auto s = __builtin_amdgcn_permlane16_swap(asu(a), asu(b), false, false); return asf(s[0]) + asf(s[1]); }
DEV float swap32_add(float a, float b) { auto s = __builtin_amdgcn_permlane32_swap(asu(a), asu(b), false, false); return asf(s[0]) + asf(s[1]); }
DEV float x16_sum(float x) { return swap16_add(x, x); }
DEV float x32_sum(float x) { return swap32_add(x, x); }
DEV float x16_max(float x) { auto s = __builtin_amdgcn_permlane16_swap(asu(x), asu(x), false, false); return fmaxf(asf(s[0]), asf(s[1])); }
DEV float x32_max(float x) { auto s = __builtin_amdgcn_permlane32_swap(asu(x), asu(x), false, false); return fmaxf(asf(s[0]), asf(s[1])); }
DEV float row_sum(float x) { x += dppf<XOR1>(x); x += dppf<XOR2>(x); x += dppf<HMIR>(x); x += dppf<ROR8>(x); return x; }
DEV float wave_sum(float x) { return x32_sum(x16_sum(row_sum(x))); }
DEV f4 mfma(v4u a, v4u b, f4 c) { return __builtin_amdgcn_mfma_f32_16x16x32_bf16(__builtin_bit_cast(bf8, a), __builtin_bit_cast(bf8, b), c, 0, 0, 0); }
#define SB() __builtin_amdgcn_sched_barrier(0)
#define BAR() asm volatile("s_waitcnt lgkmcnt(0)\n\ts_barrier" ::: "memory")
#define BAR0() asm volatile("s_barrier" ::: "memory")
#define CFENCE() asm volatile("" ::: "memory")
#define TOUCH(x) asm volatile("" : "+v"(x))

typedef __amdgpu_buffer_rsrc_t rsrc_t;
DEV rsrc_t mkrs(const void* p, unsigned bytes) { return __builtin_amdgcn_make_buffer_rsrc((void*)p, 0, bytes, 0x00020000); }
DEV v4u ldw(rsrc_t r, int voff, int soff) { return __builtin_amdgcn_raw_buffer_load_b128(r, voff, soff, W_AUX); }
DEV v4u ldx(rsrc_t r, int voff) { return __builtin_amdgcn_raw_buffer_load_b128(r, voff, 0, AUX_SC1); }
DEV void stx(rsrc_t r, int voff, v4u v) { __builtin_amdgcn_raw_buffer_store_b128(v, r, voff, 0, AUX_SC1); }
DEV unsigned tag1(float f, unsigned T) { return ((asu(f) + 0x200u) & ~TAGM) | T; }
DEV v4u tag4(f4 v, unsigned T) { return v4u{tag1(v[0], T), tag1(v[1], T), tag1(v[2], T), tag1(v[3], T)}; }
DEV unsigned bad4(v4u v, unsigned T) { return ((v[0] ^ T) | (v[1] ^ T) | (v[2] ^ T) | (v[3] ^ T)) & TAGM; }
DEV f4 val4(v4u v) { return f4{asf(v[0] & ~TAGM), asf(v[1] & ~TAGM), asf(v[2] & ~TAGM), asf(v[3] & ~TAGM)}; }

template <int I> struct IC { static constexpr int v = I; };
template <int LO, int HI, class F> DEV void sfor(F&& f) { if constexpr (LO < HI) { f(IC<LO>{}); sfor<LO + 1, HI>(f); } }
constexpr int imin(int a, int b) { return a < b ? a : b; }
constexpr int imax(int a, int b) { return a > b ? a : b; }
constexpr int upA(int t) { return t < 0 ? WA0 : imin(17 + WA2, imax(upA(t - 1), t + 1 + (t < 7 ? WA1 : WAK))); }
constexpr int upB(int t) { return t < 0 ? WB0 : imin(16 + WB2, imax(upB(t - 1), t + 1 + (t + 1 < NB0 ? WB0 : WB1))); }

struct Masks { unsigned m[4]; };
DEV v4u aop(unsigned xr, const Masks& k) { return v4u{xr & k.m[0], xr & k.m[1], xr & k.m[2], xr & k.m[3]}; }

DEV unsigned ln_own(v4u xa, v4u xb, unsigned own) {
  float v[16];
#pragma unroll
  for (int i = 0; i < 4; ++i) { v[2 * i] = bflo(xa[i]); v[2 * i + 1] = bfhi(xa[i]); v[8 + 2 * i] = bflo(xb[i]); v[9 + 2 * i] = bfhi(xb[i]); }
  float s = 0.f;
#pragma unroll
  for (int i = 0; i < 16; ++i) s += v[i];
  const float mean = wave_sum(s) * (1.f / D);
  float q = 0.f;
#pragma unroll
  for (int i = 0; i < 16; ++i) { const float d = v[i] - mean; q = fmaf(d, d, q); }
  const float var = wave_sum(q) * (1.f / D);
  const float den = rbf(__builtin_sqrtf(rbf(rbf(var) + 1e-5f)));
  const float xn = rbf(asf(own << 16) - rbf(mean)) / den;
  return pk2(xn, xn);
}

DEV unsigned voff_b(unsigned row, unsigned ch) { return 256u * row + 16u * (ch ^ (((row & 3) << 2) | ((row >> 2) & 3))); }

__global__ void __launch_bounds__(NTHR, 2)
fused_k(const u16* x, const u16* kc, const u16* vc, const u16* wq, const u16* wo, const u16* wg, const u16* wu, const u16* wd,
        u16* out, unsigned char* ws
) {
  __shared__ __attribute__((aligned(16))) unsigned char lds_raw[LDS_BYTES];
  LAS unsigned char* L = (LAS unsigned char*)lds_raw;
  const int tid = threadIdx.x, lane = tid & 63, w = __builtin_amdgcn_readfirstlane(tid >> 6);
  const int gp = lane >> 4, c = lane & 15;
  const int kg = 2 * (gp & 1) + (gp >> 1);
  const int b = blockIdx.x, xcd = b & 7, li = b >> 3;
  STAMP_DECL
  STAMP(0);
  const rsrc_t rW = mkrs(ws, WS_END);
  const rsrc_t rX = mkrs(x, D * 2);
  v4u xa = __builtin_amdgcn_raw_buffer_load_b128(rX, lane * 16, 0, 0);
  v4u xb = __builtin_amdgcn_raw_buffer_load_b128(rX, lane * 16 + 1024, 0, 0);
  const unsigned e0 = __builtin_amdgcn_raw_buffer_load_b32(rW, WS_E, 0, 0);
  unsigned T;
  Masks mk;
  auto setup = [&]() {
    T = (__builtin_amdgcn_readfirstlane(e0) + 1u) & TAGM;
    int cc = c; TOUCH(cc);
#pragma unroll
    for (int d = 0; d < 4; ++d) mk.m[d] = (cc == 2 * d) ? 0x0000ffffu : (cc == 2 * d + 1) ? 0xffff0000u : 0u;
  };
  unsigned xl;
  v4u wl[8];
  int slot, cq;

  if (li < 16) {
    const int g = xcd >> 1, j = (xcd & 1) * 16 + li;
    const int ct = j & 7, ks = j >> 3;
    const unsigned own = __builtin_amdgcn_raw_buffer_load_b16(rX, (256 * ks + 32 * w + 8 * gp + (c & 7)) * 2, 0, 0);
    cq = j & 3; slot = 8 * g + (j >> 2);
    v4u wqv[8], kv[4], vv[4], q0, q1;
    const rsrc_t rq = mkrs(wq, D * HQ * DH * 2), rk = mkrs(kc, HKV * SK * DH * 2), rv = mkrs(vc, HKV * SK * DH * 2), ro = mkrs(wo, HQ * DH * D * 2);
    const int voq = (256 * ks + 32 * w + 8 * gp) * 8192 + ((8 * g + ct) * 128 + 8 * c) * 2;
    int vokv, voo, x1off;
    auto issue = [&](auto I) {
      constexpr int k = decltype(I)::v;
      if constexpr (k < 8) wqv[k] = ldw(rq, voq, k * 8192);
      else if constexpr (k < 12) kv[k - 8] = ldw(rk, vokv + (k - 8) * 1024, 0);
      else if constexpr (k < 16) vv[k - 12] = ldw(rv, vokv + (k - 12) * 1024, 0);
      else if constexpr (k == 16) { q0 = ldx(rW, x1off); q1 = ldx(rW, x1off + 4096); }
      else wl[k - 17] = ldw(ro, voo + ((k - 17) & 1) * 256, ((k - 17) >> 1) * 8192);
    };
    sfor<0, WA0>([&](auto I) { SB(); BAR0(); issue(I); });
    SB();
    vokv = ((g * SK + (SK - WIN) + 128 * j + 16 * w) * DH) * 2 + lane * 16;
    voo = (128 * slot + 16 * w + kg) * 2048 + (256 * cq + 8 * c) * 2;
    x1off = WS_X1 + ((g * 4 + 2 * (lane >> 5)) * 1024 + w * 128) * 4 + (lane & 31) * 16;
    TOUCH(xa); TOUCH(xb);
    const unsigned xn = ln_own(xa, xb, own);
    setup();
    SB();
    STAMP(1);
    f4 acc = {0.f, 0.f, 0.f, 0.f};
    sfor<0, 8>([&](auto I) {
      constexpr int t = decltype(I)::v;
      acc = mfma(aop(dppu<NBC + t>(xn), mk), wqv[t], acc);
      SB();
      sfor<upA(t - 1), upA(t)>(issue);
      SB();
    });
    if (lane < 32) *(LAS f4*)(L + L_RED + w * 512 + (8 * c + 4 * gp) * 4) = acc;
    STAMP(2);
    BAR();
    if (w == 0) {
      const int hf = lane >> 5, ch = lane & 31;
      f4 s = *(LAS f4*)(L + L_RED + (4 * hf) * 512 + ch * 16);
#pragma unroll
      for (int i = 1; i < 4; ++i) s += *(LAS f4*)(L + L_RED + (4 * hf + i) * 512 + ch * 16);
      s[0] = x32_sum(s[0]); s[1] = x32_sum(s[1]); s[2] = x32_sum(s[2]); s[3] = x32_sum(s[3]);
      if (lane < 32) stx(rW, WS_X1 + ((g * 4 + ks) * 1024 + ct * 128) * 4 + ch * 16, tag4(s, T));
    }
    SB();
    STAMP(3);
    sfor<8, 16>([&](auto I) {
      constexpr int t = decltype(I)::v;
      if constexpr (t < 12) *(LAS v4u*)(L + L_K + voff_b(16 * w + 4 * (t - 8) + gp, c)) = kv[t - 8];
      else *(LAS v4u*)(L + L_V + voff_b(16 * w + 4 * (t - 12) + gp, c)) = vv[t - 12];
      SB();
      sfor<upA(t - 1), upA(t)>(issue);
      if constexpr (t == 11) {
#pragma unroll
        for (int i = 0; i < 4; ++i) kv[i] = *(LAS v4u*)(L + L_K + voff_b(16 * w + c, 4 * i + gp));
      }
      SB();
    });
    sfor<upA(15), 17 + WA2>(issue);
    SB();
    STAMP(4);
    {
      int spins = 0;
      unsigned bad = bad4(q0, T) | bad4(q1, T);
      if (__builtin_expect(__any(bad != 0), 0)) {
        do {
          CFENCE();
          if (bad != 0) { q0 = ldx(rW, x1off); q1 = ldx(rW, x1off + 4096); bad = bad4(q0, T) | bad4(q1, T); }
        } while (__any(bad != 0) & (++spins < SPIN_CAP));
        ++spins;
      }
      STAMPV(20, spins);
    }
    STAMP(5);
    {
      const f4 s = val4(q0) + val4(q1);
      const float a = swap32_add(s[0], s[2]), bq = swap32_add(s[1], s[3]);
      *(LAS unsigned*)(L + L_Q + w * 272 + (lane & 31) * 8 + (lane >> 5) * 4) = pk2(a, bq);
    }
    BAR();
    STAMP(12);
    f4 sc, sc2;
    {
      const LAS unsigned char* qp = L + L_Q + (c & 7) * 272 + gp * 16;
      const v4u qa = *(LAS v4u*)(qp), qb = *(LAS v4u*)(qp + 64), qc = *(LAS v4u*)(qp + 128), qd = *(LAS v4u*)(qp + 192);
      sc = mfma(kv[0], qa, f4{0.f, 0.f, 0.f, 0.f});
      sc2 = mfma(kv[1], qb, f4{0.f, 0.f, 0.f, 0.f});
      sc = mfma(kv[2], qc, sc);
      sc2 = mfma(kv[3], qd, sc2);
      sc += sc2;
    }
    {
      const unsigned a = pk2(sc[0], sc[1]), bq = pk2(sc[2], sc[3]);
      const unsigned a2 = pk2(bflo(a) * 0.08838834764831845f, bfhi(a) * 0.08838834764831845f);
      const unsigned b2 = pk2(bflo(bq) * 0.08838834764831845f, bfhi(bq) * 0.08838834764831845f);
      sc = f4{bflo(a2), bfhi(a2), bflo(b2), bfhi(b2)};
    }
    float mw = fmaxf(fmaxf(sc[0], sc[1]), fmaxf(sc[2], sc[3]));
    mw = x32_max(x16_max(mw));
    if (lane < 8) *(LAS float*)(L + L_MX + (lane * 8 + w) * 4) = mw;
    STAMP(13);
    BAR();
    float m;
    {
      const f4 a = *(LAS f4*)(L + L_MX + (c & 7) * 32), bq = *(LAS f4*)(L + L_MX + (c & 7) * 32 + 16);
      m = fmaxf(fmaxf(fmaxf(a[0], a[1]), fmaxf(a[2], a[3])), fmaxf(fmaxf(bq[0], bq[1]), fmaxf(bq[2], bq[3])));
    }
    float p[4];
#pragma unroll
    for (int r = 0; r < 4; ++r) p[r] = __builtin_amdgcn_exp2f((sc[r] - m) * LOG2E);
    if (c < 8) *(LAS v2u*)(L + L_P + c * 272 + (16 * w + 4 * gp) * 2) = v2u{pk2(p[0], p[1]), pk2(p[2], p[3])};
    float lw = (p[0] + p[1]) + (p[2] + p[3]);
    lw = x32_sum(x16_sum(lw));
    if (lane < 8) *(LAS float*)(L + L_LS + (lane * 8 + w) * 4) = lw;
    STAMP(14);
    BAR();
    f4 ov, ov2;
    {
      v4u va[4], pa[4];
#pragma unroll
      for (int t = 0; t < 4; ++t) {
        const int row = 32 * t + 8 * gp + (c >> 2), ch = 2 * w + ((c & 3) >> 1);
        const s4 a0 = __builtin_amdgcn_ds_read_tr16_b64_v4i16((LAS s4*)(L + L_V + voff_b(row, ch) + 8 * (c & 1)));
        const s4 a1 = __builtin_amdgcn_ds_read_tr16_b64_v4i16((LAS s4*)(L + L_V + voff_b(row + 4, ch) + 8 * (c & 1)));
        const v2u u0 = __builtin_bit_cast(v2u, a0), u1 = __builtin_bit_cast(v2u, a1);
        va[t] = v4u{u0[0], u0[1], u1[0], u1[1]};
        pa[t] = *(LAS v4u*)(L + L_P + (c & 7) * 272 + t * 64 + gp * 16);
      }
      ov = mfma(va[0], pa[0], f4{0.f, 0.f, 0.f, 0.f});
      ov2 = mfma(va[1], pa[1], f4{0.f, 0.f, 0.f, 0.f});
      ov = mfma(va[2], pa[2], ov);
      ov2 = mfma(va[3], pa[3], ov2);
      ov += ov2;
    }
    if (c < 8) stx(rW, WS_X2O + ((((g * 8 + c) * 8 + w) * 32 + j) * 16 + 4 * gp) * 4, tag4(ov, T));
    if (w == 0 && lane < 8) {
      const f4 a = *(LAS f4*)(L + L_LS + lane * 32), bq = *(LAS f4*)(L + L_LS + lane * 32 + 16);
      const float l = ((a[0] + a[1]) + (a[2] + a[3])) + ((bq[0] + bq[1]) + (bq[2] + bq[3]));
      stx(rW, WS_X2M + ((g * 8 + lane) * 32 + j) * 16, tag4(f4{m, l, 0.f, 0.f}, T));
    }
    STAMP(6);
    sfor<17 + WA2, 25>(issue);
    SB();
    if constexpr (X2_WAIT > 0) TOUCH(wl[X2_WAIT - 1]);
    SB();
    STAMP(17);
    {
      const int oo = WS_X2O + (slot * 8 + w) * 2048 + lane * 16;
      const int mo = WS_X2M + (slot * 32 + (lane >> 2) + 16 * (lane & 1)) * 16;
      v4u ml = ldx(rW, mo), o0 = ldx(rW, oo), o1 = ldx(rW, oo + 1024);
      unsigned bad = bad4(o0, T) | bad4(o1, T) | bad4(ml, T);
      int spins = 0;
      if (__any(bad != 0)) {
        do {
          CFENCE();
          __builtin_amdgcn_s_sleep(POLL_SLEEP);
          if (bad != 0) { ml = ldx(rW, mo); o0 = ldx(rW, oo); o1 = ldx(rW, oo + 1024); bad = bad4(o0, T) | bad4(o1, T) | bad4(ml, T); }
        } while (__any(bad != 0) & (++spins < SPIN_CAP));
        ++spins;
      }
      STAMPV(21, spins);
      STAMP(7);
      const float ms = asf(ml[0] & ~TAGM), ls = asf(ml[1] & ~TAGM);
      float M = fmaxf(ms, dppf<XOR1>(ms));
      M = fmaxf(M, dppf<ROR4>(M)); M = fmaxf(M, dppf<ROR8>(M));
      M = x32_max(x16_max(M));
      const float e = __builtin_amdgcn_exp2f((ms - M) * LOG2E);
      float Lt = e * ls;
      Lt += dppf<XOR1>(Lt); Lt += dppf<ROR4>(Lt); Lt += dppf<ROR8>(Lt);
      Lt = x32_sum(x16_sum(Lt));
      const f4 z = val4(o0) * dppf<QB0>(e) + val4(o1) * dppf<QB1>(e);
      float o = swap16_add(swap32_add(z[0], z[1]), swap32_add(z[2], z[3]));
      o += dppf<ROR4>(o); o += dppf<ROR8>(o);
      o = o / Lt;
      xl = pk2(o, o);
    }
  } else {
    const int idx = xcd * 14 + (li - 16), G = idx >> 2, j = idx & 3;
    const int kh = j & 1;
    const unsigned own = __builtin_amdgcn_raw_buffer_load_b16(rX, (512 * kh + 64 * w + lane) * 2, 0, 0);
    cq = j; slot = 32 + G;
    v4u gu[16];
    const rsrc_t rg = mkrs((j >> 1) ? wu : wg, D * DF * 2), rd = mkrs(wd, DF * D * 2);
    const int vog = (512 * kh + 64 * w + 16 * gp) * 7168 + (128 * G + 8 * c) * 2;
    int vod;
    auto issue = [&](auto I) {
      constexpr int k = decltype(I)::v;
      if constexpr (k < 16) gu[k] = ldw(rg, vog, k * 7168);
      else wl[k - 16] = ldw(rd, vod + ((k - 16) & 1) * 256, ((k - 16) >> 1) * 8192);
    };
    sfor<0, WB0>([&](auto I) { SB(); BAR0(); issue(I); });
    SB();
    vod = (128 * G + 16 * w + kg) * 2048 + (256 * cq + 8 * c) * 2;
    TOUCH(xa); TOUCH(xb);
    const unsigned xn = ln_own(xa, xb, own);
    setup();
    SB();
    STAMP(1);
    f4 acc = {0.f, 0.f, 0.f, 0.f};
    sfor<0, 16>([&](auto I) {
      constexpr int t = decltype(I)::v;
      acc = mfma(aop(dppu<NBC + t>(xn), mk), gu[t], acc);
      SB();
      sfor<upB(t - 1), upB(t)>(issue);
      SB();
    });
    sfor<upB(15), 16 + WB2>(issue);
    SB();
    if (lane < 32) *(LAS f4*)(L + L_RED + w * 512 + (8 * c + 4 * gp) * 4) = acc;
    STAMP(2);
    BAR();
    if (w == 0) {
      const int hf = lane >> 5, ch = lane & 31;
      f4 s = *(LAS f4*)(L + L_RED + (4 * hf) * 512 + ch * 16);
#pragma unroll
      for (int i = 1; i < 4; ++i) s += *(LAS f4*)(L + L_RED + (4 * hf + i) * 512 + ch * 16);
      s[0] = x32_sum(s[0]); s[1] = x32_sum(s[1]); s[2] = x32_sum(s[2]); s[3] = x32_sum(s[3]);
      if (lane < 32) stx(rW, WS_X3 + (G * 4 + j) * 512 + ch * 16, tag4(s, T));
    }
    SB();
    STAMP(3);
    sfor<16 + WB2, 24>([&](auto I) { constexpr int k = decltype(I)::v; TOUCH(wl[k - 16 - WB2]); SB(); issue(I); SB(); });
    if constexpr (X3_WAIT > 0) TOUCH(wl[X3_WAIT - 1]);
    SB();
    STAMP(4);
    const int ho = WS_X3 + (G * 4 + ((c >> 1) & 2) + (c >> 3)) * 512 + (4 * w + (c & 3)) * 16;
    v4u hv;
    int spins = 0;
    do {
      CFENCE();
      hv = ldx(rW, ho);
    } while (__any(bad4(hv, T) != 0) & (++spins < SPIN_CAP));
    STAMPV(21, spins);
    STAMP(7);
    {
      const unsigned e01 = (gp & 2) ? hv[1] : hv[0], e23 = (gp & 2) ? hv[3] : hv[2];
      const float pv = asf(((gp & 1) ? e23 : e01) & ~TAGM);
      const float s = pv + dppf<ROR8>(pv);
      const float o = dppf<ROR4>(s);
      const float gg = rbf((c & 4) ? o : s), uu = rbf((c & 4) ? s : o);
      const float y = gg * fmaf(gg * gg, 0.044715f * 0.7978845608028654f * -2.f * LOG2E, 0.7978845608028654f * -2.f * LOG2E);
      const float ge = gg * __builtin_amdgcn_rcpf(1.f + __builtin_amdgcn_exp2f(y));
      const float hh = rbf(ge) * uu;
      xl = pk2(hh, hh);
    }
  }

  {
    f4 a0 = {0.f, 0.f, 0.f, 0.f}, a1 = {0.f, 0.f, 0.f, 0.f};
    sfor<0, 4>([&](auto I) {
      constexpr int t = decltype(I)::v;
      const v4u a = aop(dppu<NBC + t>(xl), mk);
      a0 = mfma(a, wl[2 * t], a0);
      a1 = mfma(a, wl[2 * t + 1], a1);
    });
    if (lane < 32) {
      *(LAS f4*)(L + L_RED3 + w * 1024 + (8 * c + 4 * gp) * 4) = a0;
      *(LAS f4*)(L + L_RED3 + w * 1024 + 512 + (8 * c + 4 * gp) * 4) = a1;
    }
    STAMP(8);
    BAR();
    if (w == 0) {
      f4 s = *(LAS f4*)(L + L_RED3 + lane * 16);
#pragma unroll
      for (int i = 1; i < 8; ++i) s += *(LAS f4*)(L + L_RED3 + i * 1024 + lane * 16);
      stx(rW, WS_X4 + (cq * 64 + slot) * 1024 + lane * 16, tag4(s, T));
      STAMP(9);
    }
  }
  const int rid = RED_ROLE == 0 ? (li < 8 ? li * 8 + xcd : 64) : (li >= 16 && li < 24 ? (li - 16) * 8 + xcd : 64);
  if (w == 1 && rid < 64) {
    const int off = WS_X4 + ((rid >> 4) * 64 + (lane >> 2)) * 1024 + (rid & 15) * 64 + (lane & 3) * 16;
    const int off3 = lane < 48 ? off + 48 * 1024 : off;
    const int col = rid * 16 + 4 * (c & 3) + ((c >> 2) & 2) + (gp & 1);
    const unsigned xo = __builtin_amdgcn_raw_buffer_load_b16(rX, col * 2, 0, 0);
    if (X4_DELAY > 0) __builtin_amdgcn_s_sleep(X4_DELAY);
    v4u v0 = ldx(rW, off), v1 = ldx(rW, off + 16 * 1024), v2 = ldx(rW, off + 32 * 1024), v3 = ldx(rW, off3);
    unsigned b0 = bad4(v0, T), b1 = bad4(v1, T), b2 = bad4(v2, T), b3 = bad4(v3, T);
    int spins = 0;
    while (__any((b0 | b1 | b2 | b3) != 0) & (++spins < SPIN_CAP)) {
      CFENCE();
      if (b0 != 0) v0 = ldx(rW, off);
      if (b1 != 0) v1 = ldx(rW, off + 16 * 1024);
      if (b2 != 0) v2 = ldx(rW, off + 32 * 1024);
      if (b3 != 0) v3 = ldx(rW, off3);
      b0 = bad4(v0, T); b1 = bad4(v1, T); b2 = bad4(v2, T); b3 = bad4(v3, T);
    }
    STAMPV(22, spins);
    STAMP(10);
    const f4 ya = val4(v0) + val4(v1);
    f4 yb = val4(v3);
    if (lane >= 48) yb = f4{0.f, 0.f, 0.f, 0.f};
    yb += val4(v2);
    const float z0 = swap32_add(ya[0], yb[0]), z1 = swap32_add(ya[1], yb[1]), z2 = swap32_add(ya[2], yb[2]), z3 = swap32_add(ya[3], yb[3]);
    float y0 = swap16_add(z0, z1), y1 = swap16_add(z2, z3);
    y0 += dppf<ROR4>(y0); y1 += dppf<ROR4>(y1);
    y0 += dppf<ROR8>(y0); y1 += dppf<ROR8>(y1);
    const float z = (c & 8) ? y1 : y0;
    auto sw = __builtin_amdgcn_permlane32_swap(asu(z), asu(z), false, false);
    const float res = rbf(asf(xo << 16) + rbf(asf(sw[0]))) + rbf(asf(sw[1]));
    if (lane < 32 && (c & 4) == 0) out[col] = (u16)(pk2(res, res) & 0xffffu);
    if (rid == 0 && lane == 0) __builtin_amdgcn_raw_buffer_store_b32(T, rW, WS_E, 0, AUX_SC1);
    STAMP(11);
  }
  STAMP_OUT();
}

extern "C" void kernel_launch(void* const* d_in, const int* in_sizes, int n_in, void* d_out, int out_size, void* d_ws, size_t ws_size,
                              hipStream_t stream) {
  fused_k<<<NWG, NTHR, 0, stream>>>((const u16*)d_in[0], (const u16*)d_in[1], (const u16*)d_in[2], (const u16*)d_in[3], (const u16*)d_in[4],
                                    (const u16*)d_in[6], (const u16*)d_in[7], (const u16*)d_in[8], (u16*)d_out, (unsigned char*)d_ws);
}
```

```cpp
#define AMP 2
#define AMPB 8
#define W_AUX 2
#define B_SLEEP 40
#include <hip/hip_runtime.h>
#include <hip/hip_bf16.h>
#include <stdint.h>

typedef unsigned short u16;
typedef unsigned long long u64;
typedef unsigned v4u __attribute__((ext_vector_type(4)));
typedef unsigned v2u __attribute__((ext_vector_type(2)));
typedef float f4 __attribute__((ext_vector_type(4)));
typedef float f2 __attribute__((ext_vector_type(2)));
typedef __bf16 bf8 __attribute__((ext_vector_type(8)));
typedef __bf16 bf2 __attribute__((ext_vector_type(2)));
typedef short s4 __attribute__((ext_vector_type(4)));
#define LAS __attribute__((address_space(3)))
#define DEV __device__ __forceinline__

#define WA0 6
#define WA1 6
#define WAK 5
#define X1_AT 15
#define WA2A 2
#define WA2 6
#define WB0 1
#define WB1 5
#define WB2 5
#define X2_WAIT 8
#define X2_SLEEP 0
#define X3_WAIT 6
#define X4_DELAY 8
#define POLL_SLEEP 1
#define RED_ROLE 1

constexpr int D = 1024, HQ = 32, HKV = 4, SK = 32768, DH = 128, WIN = 4096, DF = 3584;
constexpr int NWG = 240, NTHR = 512;
constexpr int AUX_SC1 = 16;
constexpr unsigned TAGM = 0x3FFu;
constexpr int SPIN_CAP = 1 << 16;
constexpr float LOG2E = 1.4426950408889634f;

constexpr int WS_E = 0;
constexpr int WS_X1 = 4096;
constexpr int WS_X2O = WS_X1 + 65536;
constexpr int WS_X2M = WS_X2O + 524288;
constexpr int WS_X3 = WS_X2M + 16384;
constexpr int WS_X4 = WS_X3 + 65536;
constexpr int WS_END = WS_X4 + 262144;

constexpr int L_V = 0;
constexpr int L_K = 32768;
constexpr int L_RED = 65536;
constexpr int L_Q = L_RED + 4096;
constexpr int L_P = L_Q + 8 * 272;
constexpr int L_MX = L_P + 8 * 272;
constexpr int L_LS = L_MX + 256;
constexpr int L_RED3 = L_LS + 256;
constexpr int L_END = L_RED3 + 8192;
constexpr int LDS_BYTES = 84 * 1024;
static_assert(L_END <= LDS_BYTES, "lds");

#define STAMP(i) do { if ((i) == 0 || (i) == AMP) asm volatile("s_memrealtime %0\n\ts_waitcnt lgkmcnt(0)" : "=s"(st_[(i) == 0 ? 0 : 1]) :: "memory"); } while (0)
#define STAMPV(i, v) do {} while (0)
#define STAMP_DECL u64 st_[2] = {0,0};
#define STAMP_OUT() do { if (w == 1 && b == AMPB) { u64 now_; const u64 until_ = st_[0] + 20 * (st_[1] - st_[0]); do { __builtin_amdgcn_s_sleep(1); asm volatile("s_memrealtime %0\n\ts_waitcnt lgkmcnt(0)" : "=s"(now_) :: "memory"); } while (now_ < until_); } } while (0)

DEV float asf(unsigned u) { return __builtin_bit_cast(float, u); }
DEV unsigned asu(float f) { return __builtin_bit_cast(unsigned, f); }
DEV float bflo(unsigned w) { return asf(w << 16); }
DEV float bfhi(unsigned w) { return asf(w & 0xffff0000u); }
DEV unsigned pk2(float lo, float hi) { f2 v = {lo, hi}; return __builtin_bit_cast(unsigned, __builtin_convertvector(v, bf2)); }
DEV float rbf(float f) { return asf(pk2(f, f) & 0xffff0000u); }
template <int C> DEV float dppf(float x) { return asf(__builtin_amdgcn_mov_dpp(asu(x), C, 0xf, 0xf, true)); }
template <int C> DEV unsigned dppu(unsigned x) { return __builtin_amdgcn_mov_dpp(x, C, 0xf, 0xf, true); }
constexpr int XOR1 = 0xB1, XOR2 = 0x4E, HMIR = 0x141, ROR4 = 0x124, ROR8 = 0x128, NBC = 0x150, QB0 = 0x00, QB1 = 0x55;
DEV float swap16_add(float a, float b) { auto s = __builtin_amdgcn_permlane16_swap(asu(a), asu(b), false, false); return asf(s[0]) + asf(s[1]); }
DEV float swap32_add(float a, float b) { auto s = __builtin_amdgcn_permlane32_swap(asu(a), asu(b), false, false); return asf(s[0]) + asf(s[1]); }
DEV float x16_sum(float x) { return swap16_add(x, x); }
DEV float x32_sum(float x) { return swap32_add(x, x); }
DEV float x16_max(float x) { auto s = __builtin_amdgcn_permlane16_swap(asu(x), asu(x), false, false); return fmaxf(asf(s[0]), asf(s[1])); }
DEV float x32_max(float x) { auto s = __builtin_amdgcn_permlane32_swap(asu(x), asu(x), false, false); return fmaxf(asf(s[0]), asf(s[1])); }
DEV float row_sum(float x) { x += dppf<XOR1>(x); x += dppf<XOR2>(x); x += dppf<HMIR>(x); x += dppf<ROR8>(x); return x; }
DEV float wave_sum(float x) { return x32_sum(x16_sum(row_sum(x))); }
DEV f4 mfma(v4u a, v4u b, f4 c) { return __builtin_amdgcn_mfma_f32_16x16x32_bf16(__builtin_bit_cast(bf8, a), __builtin_bit_cast(bf8, b), c, 0, 0, 0); }
#define SB() __builtin_amdgcn_sched_barrier(0)
#define BAR() asm volatile("s_waitcnt lgkmcnt(0)\n\ts_barrier" ::: "memory")
#define BAR0() asm volatile("s_barrier" ::: "memory")
#define CFENCE() asm volatile("" ::: "memory")
#define TOUCH(x) asm volatile("" : "+v"(x))

typedef __amdgpu_buffer_rsrc_t rsrc_t;
DEV rsrc_t mkrs(const void* p, unsigned bytes) { return __builtin_amdgcn_make_buffer_rsrc((void*)p, 0, bytes, 0x00020000); }
DEV v4u ldw(rsrc_t r, int voff, int soff) { return __builtin_amdgcn_raw_buffer_load_b128(r, voff, soff, W_AUX); }
DEV v4u ldx(rsrc_t r, int voff) { return __builtin_amdgcn_raw_buffer_load_b128(r, voff, 0, AUX_SC1); }
DEV void stx(rsrc_t r, int voff, v4u v) { __builtin_amdgcn_raw_buffer_store_b128(v, r, voff, 0, AUX_SC1); }
DEV unsigned tag1(float f, unsigned T) { return ((asu(f) + 0x200u) & ~TAGM) | T; }
DEV v4u tag4(f4 v, unsigned T) { return v4u{tag1(v[0], T), tag1(v[1], T), tag1(v[2], T), tag1(v[3], T)}; }
DEV unsigned bad4(v4u v, unsigned T) { return ((v[0] ^ T) | (v[1] ^ T) | (v[2] ^ T) | (v[3] ^ T)) & TAGM; }
DEV f4 val4(v4u v) { return f4{asf(v[0] & ~TAGM), asf(v[1] & ~TAGM), asf(v[2] & ~TAGM), asf(v[3] & ~TAGM)}; }

template <int I> struct IC { static constexpr int v = I; };
template <int LO, int HI, class F> DEV void sfor(F&& f) { if constexpr (LO < HI) { f(IC<LO>{}); sfor<LO + 1, HI>(f); } }
constexpr int imin(int a, int b) { return a < b ? a : b; }
constexpr int imax(int a, int b) { return a > b ? a : b; }
constexpr int upA(int t) { return t < 0 ? imin(8, imax(WA0, WA1 + 2)) : t >= X1_AT ? imin(17 + WA2A, imax(17, t + 1 + WAK)) : imin(16, imax(upA(t - 1), t + 1 + (t < 7 ? WA1 : WAK))); }
constexpr int upB(int t) { return t < 0 ? imax(WB0, WB1) : imin(16 + WB2, imax(upB(t - 1), t + 1 + WB1)); }

struct Masks { unsigned m[4]; };
DEV v4u aop(unsigned xr, const Masks& k) { return v4u{xr & k.m[0], xr & k.m[1], xr & k.m[2], xr & k.m[3]}; }

DEV unsigned ln_own(v4u xa, v4u xb, unsigned own) {
  float v[16];
#pragma unroll
  for (int i = 0; i < 4; ++i) { v[2 * i] = bflo(xa[i]); v[2 * i + 1] = bfhi(xa[i]); v[8 + 2 * i] = bflo(xb[i]); v[9 + 2 * i] = bfhi(xb[i]); }
  float s = 0.f;
#pragma unroll
  for (int i = 0; i < 16; ++i) s += v[i];
  const float mean = wave_sum(s) * (1.f / D);
  float q = 0.f;
#pragma unroll
  for (int i = 0; i < 16; ++i) { const float d = v[i] - mean; q = fmaf(d, d, q); }
  const float var = wave_sum(q) * (1.f / D);
  const float den = rbf(__builtin_sqrtf(rbf(rbf(var) + 1e-5f)));
  const float xn = rbf(asf(own << 16) - rbf(mean)) / den;
  return pk2(xn, xn);
}

DEV unsigned voff_b(unsigned row, unsigned ch) { return 256u * row + 16u * (ch ^ (((row & 3) << 2) | ((row >> 2) & 3))); }

__global__ void __launch_bounds__(NTHR, 2)
fused_k(const u16* x, const u16* kc, const u16* vc, const u16* wq, const u16* wo, const u16* wg, const u16* wu, const u16* wd,
        u16* out, unsigned char* ws
) {
  __shared__ __attribute__((aligned(16))) unsigned char lds_raw[LDS_BYTES];
  LAS unsigned char* L = (LAS unsigned char*)lds_raw;
  const int tid = threadIdx.x, lane = tid & 63, w = __builtin_amdgcn_readfirstlane(tid >> 6);
  const int gp = lane >> 4, c = lane & 15;
  const int kg = 2 * (gp & 1) + (gp >> 1);
  const int b = blockIdx.x, xcd = b & 7, li = b >> 3;
  STAMP_DECL
  STAMP(0);
  const rsrc_t rW = mkrs(ws, WS_END);
  const rsrc_t rX = mkrs(x, D * 2);
  v4u xa = __builtin_amdgcn_raw_buffer_load_b128(rX, lane * 16, 0, 0);
  v4u xb = __builtin_amdgcn_raw_buffer_load_b128(rX, lane * 16 + 1024, 0, 0);
  const unsigned e0 = __builtin_amdgcn_raw_buffer_load_b32(rW, WS_E, 0, 0);
  unsigned T;
  Masks mk;
  auto setup = [&]() {
    T = (__builtin_amdgcn_readfirstlane(e0) + 1u) & TAGM;
    int cc = c; TOUCH(cc);
#pragma unroll
    for (int d = 0; d < 4; ++d) mk.m[d] = (cc == 2 * d) ? 0x0000ffffu : (cc == 2 * d + 1) ? 0xffff0000u : 0u;
  };
  unsigned xl;
  v4u wl[8];
  int slot, cq;

  if (li < 16) {
    const int g = xcd >> 1, j = (xcd & 1) * 16 + li;
    const int ct = j & 7, ks = j >> 3;
    const unsigned own = __builtin_amdgcn_raw_buffer_load_b16(rX, (256 * ks + 32 * w + 8 * gp + (c & 7)) * 2, 0, 0);
    cq = j & 3; slot = 8 * g + (j >> 2);
    v4u wqv[8], kv[4], vv[4], q0, q1;
    const rsrc_t rq = mkrs(wq, D * HQ * DH * 2), rk = mkrs(kc, HKV * SK * DH * 2), rv = mkrs(vc, HKV * SK * DH * 2), ro = mkrs(wo, HQ * DH * D * 2);
    const int voq = (256 * ks + 32 * w + 8 * gp) * 8192 + ((8 * g + ct) * 128 + 8 * c) * 2;
    int vokv, voo, x1off;
    auto issue = [&](auto I) {
      constexpr int k = decltype(I)::v;
      if constexpr (k < 8) wqv[k] = ldw(rq, voq, k * 8192);
      else if constexpr (k < 12) kv[k - 8] = ldw(rk, vokv + (k - 8) * 1024, 0);
      else if constexpr (k < 16) vv[k - 12] = ldw(rv, vokv + (k - 12) * 1024, 0);
      else if constexpr (k == 16) { q0 = ldx(rW, x1off); q1 = ldx(rW, x1off + 4096); }
      else wl[k - 17] = ldw(ro, voo + ((k - 17) & 1) * 256, ((k - 17) >> 1) * 8192);
    };
    sfor<0, WA0>([&](auto I) { SB(); BAR0(); issue(I); });
    SB();
    vokv = ((g * SK + (SK - WIN) + 128 * j + 16 * w) * DH) * 2 + lane * 16;
    voo = (128 * slot + 16 * w + kg) * 2048 + (256 * cq + 8 * c) * 2;
    x1off = WS_X1 + ((g * 4 + 2 * (lane >> 5)) * 1024 + w * 128) * 4 + (lane & 31) * 16;
    TOUCH(xa); TOUCH(xb);
    const unsigned xn = ln_own(xa, xb, own);
    SB();
    sfor<WA0, upA(-1)>(issue);
    SB();
    setup();
    SB();
    STAMP(1);
    f4 acc = {0.f, 0.f, 0.f, 0.f};
    sfor<0, 8>([&](auto I) {
      constexpr int t = decltype(I)::v;
      acc = mfma(aop(dppu<NBC + t>(xn), mk), wqv[t], acc);
      SB();
      sfor<upA(t - 1), upA(t)>(issue);
      SB();
    });
    if (lane < 32) *(LAS f4*)(L + L_RED + w * 512 + (8 * c + 4 * gp) * 4) = acc;
    STAMP(2);
    BAR();
    if (w == 0) {
      const int hf = lane >> 5, ch = lane & 31;
      f4 s = *(LAS f4*)(L + L_RED + (4 * hf) * 512 + ch * 16);
#pragma unroll
      for (int i = 1; i < 4; ++i) s += *(LAS f4*)(L + L_RED + (4 * hf + i) * 512 + ch * 16);
      s[0] = x32_sum(s[0]); s[1] = x32_sum(s[1]); s[2] = x32_sum(s[2]); s[3] = x32_sum(s[3]);
      if (lane < 32) stx(rW, WS_X1 + ((g * 4 + ks) * 1024 + ct * 128) * 4 + ch * 16, tag4(s, T));
    }
    SB();
    STAMP(3);
    sfor<8, 16>([&](auto I) {
      constexpr int t = decltype(I)::v;
      if constexpr (t < 12) *(LAS v4u*)(L + L_K + voff_b(16 * w + 4 * (t - 8) + gp, c)) = kv[t - 8];
      else *(LAS v4u*)(L + L_V + voff_b(16 * w + 4 * (t - 12) + gp, c)) = vv[t - 12];
      SB();
      sfor<upA(t - 1), upA(t)>(issue);
      if constexpr (t == 11) {
#pragma unroll
        for (int i = 0; i < 4; ++i) kv[i] = *(LAS v4u*)(L + L_K + voff_b(16 * w + c, 4 * i + gp));
      }
      SB();
    });
    sfor<upA(15), 17 + WA2A>(issue);
    SB();
    STAMP(4);
    {
      int spins = 0;
      unsigned bad = bad4(q0, T) | bad4(q1, T);
      if (__builtin_expect(__any(bad != 0), 0)) {
        do {
          CFENCE();
          if (bad != 0) { q0 = ldx(rW, x1off); q1 = ldx(rW, x1off + 4096); bad = bad4(q0, T) | bad4(q1, T); }
        } while (__any(bad != 0) & (++spins < SPIN_CAP));
        ++spins;
      }
      STAMPV(20, spins);
    }
    STAMP(5);
    SB();
    sfor<17 + WA2A, 17 + WA2>(issue);
    SB();
    {
      const f4 s = val4(q0) + val4(q1);
      const float a = swap32_add(s[0], s[2]), bq = swap32_add(s[1], s[3]);
      *(LAS unsigned*)(L + L_Q + w * 272 + (lane & 31) * 8 + (lane >> 5) * 4) = pk2(a, bq);
    }
    BAR();
    STAMP(12);
    f4 sc, sc2;
    {
      const LAS unsigned char* qp = L + L_Q + (c & 7) * 272 + gp * 16;
      const v4u qa = *(LAS v4u*)(qp), qb = *(LAS v4u*)(qp + 64), qc = *(LAS v4u*)(qp + 128), qd = *(LAS v4u*)(qp + 192);
      sc = mfma(kv[0], qa, f4{0.f, 0.f, 0.f, 0.f});
      sc2 = mfma(kv[1], qb, f4{0.f, 0.f, 0.f, 0.f});
      sc = mfma(kv[2], qc, sc);
      sc2 = mfma(kv[3], qd, sc2);
      sc += sc2;
    }
    {
      const unsigned a = pk2(sc[0], sc[1]), bq = pk2(sc[2], sc[3]);
      const unsigned a2 = pk2(bflo(a) * 0.08838834764831845f, bfhi(a) * 0.08838834764831845f);
      const unsigned b2 = pk2(bflo(bq) * 0.08838834764831845f, bfhi(bq) * 0.08838834764831845f);
      sc = f4{bflo(a2), bfhi(a2), bflo(b2), bfhi(b2)};
    }
    float mw = fmaxf(fmaxf(sc[0], sc[1]), fmaxf(sc[2], sc[3]));
    mw = x32_max(x16_max(mw));
    if (lane < 8) *(LAS float*)(L + L_MX + (lane * 8 + w) * 4) = mw;
    STAMP(13);
    BAR();
    float m;
    {
      const f4 a = *(LAS f4*)(L + L_MX + (c & 7) * 32), bq = *(LAS f4*)(L + L_MX + (c & 7) * 32 + 16);
      m = fmaxf(fmaxf(fmaxf(a[0], a[1]), fmaxf(a[2], a[3])), fmaxf(fmaxf(bq[0], bq[1]), fmaxf(bq[2], bq[3])));
    }
    float p[4];
#pragma unroll
    for (int r = 0; r < 4; ++r) p[r] = __builtin_amdgcn_exp2f((sc[r] - m) * LOG2E);
    if (c < 8) *(LAS v2u*)(L + L_P + c * 272 + (16 * w + 4 * gp) * 2) = v2u{pk2(p[0], p[1]), pk2(p[2], p[3])};
    float lw = (p[0] + p[1]) + (p[2] + p[3]);
    lw = x32_sum(x16_sum(lw));
    if (lane < 8) *(LAS float*)(L + L_LS + (lane * 8 + w) * 4) = lw;
    STAMP(14);
    BAR();
    f4 ov, ov2;
    {
      v4u va[4], pa[4];
#pragma unroll
      for (int t = 0; t < 4; ++t) {
        const int row = 32 * t + 8 * gp + (c >> 2), ch = 2 * w + ((c & 3) >> 1);
        const s4 a0 = __builtin_amdgcn_ds_read_tr16_b64_v4i16((LAS s4*)(L + L_V + voff_b(row, ch) + 8 * (c & 1)));
        const s4 a1 = __builtin_amdgcn_ds_read_tr16_b64_v4i16((LAS s4*)(L + L_V + voff_b(row + 4, ch) + 8 * (c & 1)));
        const v2u u0 = __builtin_bit_cast(v2u, a0), u1 = __builtin_bit_cast(v2u, a1);
        va[t] = v4u{u0[0], u0[1], u1[0], u1[1]};
        pa[t] = *(LAS v4u*)(L + L_P + (c & 7) * 272 + t * 64 + gp * 16);
      }
      ov = mfma(va[0], pa[0], f4{0.f, 0.f, 0.f, 0.f});
      ov2 = mfma(va[1], pa[1], f4{0.f, 0.f, 0.f, 0.f});
      ov = mfma(va[2], pa[2], ov);
      ov2 = mfma(va[3], pa[3], ov2);
      ov += ov2;
    }
    if (c < 8) stx(rW, WS_X2O + ((((g * 8 + c) * 8 + w) * 32 + j) * 16 + 4 * gp) * 4, tag4(ov, T));
    if (w == 0 && lane < 8) {
      const f4 a = *(LAS f4*)(L + L_LS + lane * 32), bq = *(LAS f4*)(L + L_LS + lane * 32 + 16);
      const float l = ((a[0] + a[1]) + (a[2] + a[3])) + ((bq[0] + bq[1]) + (bq[2] + bq[3]));
      stx(rW, WS_X2M + ((g * 8 + lane) * 32 + j) * 16, tag4(f4{m, l, 0.f, 0.f}, T));
    }
    STAMP(6);
    sfor<17 + WA2, 25>(issue);
    SB();
    if constexpr (X2_WAIT > 0) TOUCH(wl[X2_WAIT - 1]);
    SB();
    if (X2_SLEEP > 0) __builtin_amdgcn_s_sleep(X2_SLEEP);
    STAMP(17);
    {
      const int oo = WS_X2O + (slot * 8 + w) * 2048 + lane * 16;
      const int mo = WS_X2M + (slot * 32 + (lane >> 2) + 16 * (lane & 1)) * 16;
      v4u ml = ldx(rW, mo), o0 = ldx(rW, oo), o1 = ldx(rW, oo + 1024);
      unsigned bad = bad4(o0, T) | bad4(o1, T) | bad4(ml, T);
      int spins = 0;
      if (__any(bad != 0)) {
        do {
          CFENCE();
          __builtin_amdgcn_s_sleep(POLL_SLEEP);
          if (bad != 0) { ml = ldx(rW, mo); o0 = ldx(rW, oo); o1 = ldx(rW, oo + 1024); bad = bad4(o0, T) | bad4(o1, T) | bad4(ml, T); }
        } while (__any(bad != 0) & (++spins < SPIN_CAP));
        ++spins;
      }
      STAMPV(21, spins);
      STAMP(7);
      const float ms = asf(ml[0] & ~TAGM), ls = asf(ml[1] & ~TAGM);
      float M = fmaxf(ms, dppf<XOR1>(ms));
      M = fmaxf(M, dppf<ROR4>(M)); M = fmaxf(M, dppf<ROR8>(M));
      M = x32_max(x16_max(M));
      const float e = __builtin_amdgcn_exp2f((ms - M) * LOG2E);
      float Lt = e * ls;
      Lt += dppf<XOR1>(Lt); Lt += dppf<ROR4>(Lt); Lt += dppf<ROR8>(Lt);
      Lt = x32_sum(x16_sum(Lt));
      const f4 z = val4(o0) * dppf<QB0>(e) + val4(o1) * dppf<QB1>(e);
      float o = swap16_add(swap32_add(z[0], z[1]), swap32_add(z[2], z[3]));
      o += dppf<ROR4>(o); o += dppf<ROR8>(o);
      o = o / Lt;
      xl = pk2(o, o);
    }
  } else {
    const int idx = xcd * 14 + (li - 16), G = idx >> 2, j = idx & 3;
    const int kh = j & 1;
    const unsigned own = __builtin_amdgcn_raw_buffer_load_b16(rX, (512 * kh + 64 * w + lane) * 2, 0, 0);
    cq = j; slot = 32 + G;
    v4u gu[16];
    const rsrc_t rg = mkrs((j >> 1) ? wu : wg, D * DF * 2), rd = mkrs(wd, DF * D * 2);
    const int vog = (512 * kh + 64 * w + 16 * gp) * 7168 + (128 * G + 8 * c) * 2;
    int vod;
    auto issue = [&](auto I) {
      constexpr int k = decltype(I)::v;
      if constexpr (k < 16) gu[k] = ldw(rg, vog, k * 7168);
      else wl[k - 16] = ldw(rd, vod + ((k - 16) & 1) * 256, ((k - 16) >> 1) * 8192);
    };
    sfor<0, WB0>([&](auto I) { SB(); BAR0(); issue(I); });
    SB();
    vod = (128 * G + 16 * w + kg) * 2048 + (256 * cq + 8 * c) * 2;
    TOUCH(xa); TOUCH(xb);
    const unsigned xn = ln_own(xa, xb, own);
    setup();
    SB();
    if (B_SLEEP > 0) __builtin_amdgcn_s_sleep(B_SLEEP);
    sfor<WB0, upB(-1)>([&](auto I) { SB(); BAR0(); issue(I); });
    SB();
    STAMP(1);
    f4 acc = {0.f, 0.f, 0.f, 0.f};
    sfor<0, 16>([&](auto I) {
      constexpr int t = decltype(I)::v;
      acc = mfma(aop(dppu<NBC + t>(xn), mk), gu[t], acc);
      SB();
      sfor<upB(t - 1), upB(t)>(issue);
      SB();
    });
    sfor<upB(15), 16 + WB2>(issue);
    SB();
    if (lane < 32) *(LAS f4*)(L + L_RED + w * 512 + (8 * c + 4 * gp) * 4) = acc;
    STAMP(2);
    BAR();
    if (w == 0) {
      const int hf = lane >> 5, ch = lane & 31;
      f4 s = *(LAS f4*)(L + L_RED + (4 * hf) * 512 + ch * 16);
#pragma unroll
      for (int i = 1; i < 4; ++i) s += *(LAS f4*)(L + L_RED + (4 * hf + i) * 512 + ch * 16);
      s[0] = x32_sum(s[0]); s[1] = x32_sum(s[1]); s[2] = x32_sum(s[2]); s[3] = x32_sum(s[3]);
      if (lane < 32) stx(rW, WS_X3 + (G * 4 + j) * 512 + ch * 16, tag4(s, T));
    }
    SB();
    STAMP(3);
    sfor<16 + WB2, 24>([&](auto I) { constexpr int k = decltype(I)::v; TOUCH(wl[k - 16 - WB2]); SB(); issue(I); SB(); });
    if constexpr (X3_WAIT > 0) TOUCH(wl[X3_WAIT - 1]);
    SB();
    STAMP(4);
    const int ho = WS_X3 + (G * 4 + ((c >> 1) & 2) + (c >> 3)) * 512 + (4 * w + (c & 3)) * 16;
    v4u hv;
    int spins = 0;
    do {
      CFENCE();
      hv = ldx(rW, ho);
    } while (__any(bad4(hv, T) != 0) & (++spins < SPIN_CAP));
    STAMPV(21, spins);
    STAMP(7);
    {
      const unsigned e01 = (gp & 2) ? hv[1] : hv[0], e23 = (gp & 2) ? hv[3] : hv[2];
      const float pv = asf(((gp & 1) ? e23 : e01) & ~TAGM);
      const float s = pv + dppf<ROR8>(pv);
      const float o = dppf<ROR4>(s);
      const float gg = rbf((c & 4) ? o : s), uu = rbf((c & 4) ? s : o);
      const float y = gg * fmaf(gg * gg, 0.044715f * 0.7978845608028654f * -2.f * LOG2E, 0.7978845608028654f * -2.f * LOG2E);
      const float ge = gg * __builtin_amdgcn_rcpf(1.f + __builtin_amdgcn_exp2f(y));
      const float hh = rbf(ge) * uu;
      xl = pk2(hh, hh);
    }
  }

  {
    f4 a0 = {0.f, 0.f, 0.f, 0.f}, a1 = {0.f, 0.f, 0.f, 0.f};
    sfor<0, 4>([&](auto I) {
      constexpr int t = decltype(I)::v;
      const v4u a = aop(dppu<NBC + t>(xl), mk);
      a0 = mfma(a, wl[2 * t], a0);
      a1 = mfma(a, wl[2 * t + 1], a1);
    });
    if (lane < 32) {
      *(LAS f4*)(L + L_RED3 + w * 1024 + (8 * c + 4 * gp) * 4) = a0;
      *(LAS f4*)(L + L_RED3 + w * 1024 + 512 + (8 * c + 4 * gp) * 4) = a1;
    }
    STAMP(8);
    BAR();
    if (w == 0) {
      f4 s = *(LAS f4*)(L + L_RED3 + lane * 16);
#pragma unroll
      for (int i = 1; i < 8; ++i) s += *(LAS f4*)(L + L_RED3 + i * 1024 + lane * 16);
      stx(rW, WS_X4 + (cq * 64 + slot) * 1024 + lane * 16, tag4(s, T));
      STAMP(9);
    }
  }
  const int rid = RED_ROLE == 0 ? (li < 8 ? li * 8 + xcd : 64) : (li >= 16 && li < 24 ? (li - 16) * 8 + xcd : 64);
  if (w == 1 && rid < 64) {
    const int off = WS_X4 + ((rid >> 4) * 64 + (lane >> 2)) * 1024 + (rid & 15) * 64 + (lane & 3) * 16;
    const int off3 = lane < 48 ? off + 48 * 1024 : off;
    const int col = rid * 16 + 4 * (c & 3) + ((c >> 2) & 2) + (gp & 1);
    const unsigned xo = __builtin_amdgcn_raw_buffer_load_b16(rX, col * 2, 0, 0);
    if (X4_DELAY > 0) __builtin_amdgcn_s_sleep(X4_DELAY);
    v4u v0 = ldx(rW, off), v1 = ldx(rW, off + 16 * 1024), v2 = ldx(rW, off + 32 * 1024), v3 = ldx(rW, off3);
    unsigned bad = bad4(v0, T) | bad4(v1, T) | bad4(v2, T) | bad4(v3, T);
    int spins = 0;
    if (__any(bad != 0)) {
      do {
        CFENCE();
        if (bad != 0) {
          v0 = ldx(rW, off); v1 = ldx(rW, off + 16 * 1024); v2 = ldx(rW, off + 32 * 1024); v3 = ldx(rW, off3);
          bad = bad4(v0, T) | bad4(v1, T) | bad4(v2, T) | bad4(v3, T);
        }
      } while (__any(bad != 0) & (++spins < SPIN_CAP));
      ++spins;
    }
    STAMPV(22, spins);
    STAMP(10);
    const f4 ya = val4(v0) + val4(v1);
    f4 yb = val4(v3);
    if (lane >= 48) yb = f4{0.f, 0.f, 0.f, 0.f};
    yb += val4(v2);
    const float z0 = swap32_add(ya[0], yb[0]), z1 = swap32_add(ya[1], yb[1]), z2 = swap32_add(ya[2], yb[2]), z3 = swap32_add(ya[3], yb[3]);
    float y0 = swap16_add(z0, z1), y1 = swap16_add(z2, z3);
    y0 += dppf<ROR4>(y0); y1 += dppf<ROR4>(y1);
    y0 += dppf<ROR8>(y0); y1 += dppf<ROR8>(y1);
    const float z = (c & 8) ? y1 : y0;
    auto sw = __builtin_amdgcn_permlane32_swap(asu(z), asu(z), false, false);
    const float res = rbf(asf(xo << 16) + rbf(asf(sw[0]))) + rbf(asf(sw[1]));
    if (lane < 32 && (c & 4) == 0) out[col] = (u16)(pk2(res, res) & 0xffffu);
    if (rid == 0 && lane == 0) __builtin_amdgcn_raw_buffer_store_b32(T, rW, WS_E, 0, AUX_SC1);
    STAMP(11);
  }
  STAMP_OUT();
}

extern "C" void kernel_launch(void* const* d_in, const int* in_sizes, int n_in, void* d_out, int out_size, void* d_ws, size_t ws_size,
                              hipStream_t stream) {
  fused_k<<<NWG, NTHR, 0, stream>>>((const u16*)d_in[0], (const u16*)d_in[1], (const u16*)d_in[2], (const u16*)d_in[3], (const u16*)d_in[4],
                                    (const u16*)d_in[6], (const u16*)d_in[7], (const u16*)d_in[8], (u16*)d_out, (unsigned char*)d_ws);
}
```

```cpp
#include <hip/hip_runtime.h>
#include <hip/hip_bf16.h>
#include <stdint.h>

typedef unsigned short u16;
typedef unsigned long long u64;
typedef unsigned v4u __attribute__((ext_vector_type(4)));
typedef unsigned v2u __attribute__((ext_vector_type(2)));
typedef float f4 __attribute__((ext_vector_type(4)));
typedef float f2 __attribute__((ext_vector_type(2)));
typedef __bf16 bf8 __attribute__((ext_vector_type(8)));
typedef __bf16 bf2 __attribute__((ext_vector_type(2)));
typedef unsigned long long u64x8 __attribute__((ext_vector_type(8)));
typedef unsigned long long u64x2 __attribute__((ext_vector_type(2)));
typedef short s4 __attribute__((ext_vector_type(4)));
#define LAS __attribute__((address_space(3)))
#define GAS __attribute__((address_space(1)))
#define CAS __attribute__((address_space(4)))
#define DEV __device__ __forceinline__
#define INL __attribute__((always_inline))

constexpr int W_AUX = 2;
constexpr int WA0 = 5;
constexpr int WAL = 6;
constexpr int WA1 = 4;
constexpr int WAK = 4;
constexpr int X1_AT = 15;
constexpr int WA2 = 3;
constexpr int WA2M = 4;
constexpr int WB0 = 3;
constexpr int WBE = 3;
constexpr int WBSW = 8;
constexpr int WB1 = 5;
constexpr int WB2 = 2;
#define LATE_A {-1, -1, -1, -1, 0, 1, 2, 3}
#define LATE_B {-1, -1, -1, 0, 1, 2, 3, 4}
constexpr int NCOM = 3;
constexpr int POLL_SLEEP = 8;

constexpr int D = 1024, HQ = 32, HKV = 4, SK = 32768, DH = 128, WIN = 4096, DF = 3584;
constexpr int NWG = 241, NTHR = 512;
constexpr int AUX_SC1 = 16;
constexpr unsigned TAGM = 0x3FFu;
constexpr int SPIN_CAP = 1 << 16;
constexpr float LOG2E = 1.4426950408889634f;

constexpr int WS_E = 0;
constexpr int WS_X1 = 4096;
constexpr int WS_X2O = WS_X1 + 2 * 65536;
constexpr int WS_X2M = WS_X2O + 2 * 524288;
constexpr int WS_X3 = WS_X2M + 2 * 16384;
constexpr int WS_ACC = WS_X3 + 2 * 229376;
constexpr int WS_FLAG = WS_ACC + 2 * 16384;
constexpr int WS_END = WS_FLAG + 256;
constexpr __host__ __device__ int px(int o) { return ((o & ~127) << 1) + (o & 127); }

constexpr int L_V = 0;
constexpr int L_K = 32768;
constexpr int L_RED = 65536;
constexpr int L_Q = L_RED + 4608;
constexpr int L_P = L_Q + 8 * 272;
constexpr int L_MX = L_P + 8 * 272;
constexpr int L_RED3 = L_MX + 256;
constexpr int L_LN = L_RED3 + 8192;
constexpr int L_END = L_LN + 4096;
constexpr int LDS_BYTES = 88 * 1024;
static_assert(L_END <= LDS_BYTES, "lds");

DEV float asf(unsigned u) { return __builtin_bit_cast(float, u); }
DEV unsigned asu(float f) { return __builtin_bit_cast(unsigned, f); }
DEV float bflo(unsigned w) { return asf(w << 16); }
DEV float bfhi(unsigned w) { return asf(w & 0xffff0000u); }
DEV unsigned pk2(float lo, float hi) { f2 v = {lo, hi}; return __builtin_bit_cast(unsigned, __builtin_convertvector(v, bf2)); }
DEV float rbf(float f) { return asf(pk2(f, f) & 0xffff0000u); }
template <int C> DEV float dppf(float x) { return asf(__builtin_amdgcn_mov_dpp(asu(x), C, 0xf, 0xf, true)); }
template <int C> DEV unsigned dppu(unsigned x) { return __builtin_amdgcn_mov_dpp(x, C, 0xf, 0xf, true); }
constexpr int XOR1 = 0xB1, XOR2 = 0x4E, HMIR = 0x141, ROR4 = 0x124, ROR8 = 0x128, NBC = 0x150, QB0 = 0x00, QB1 = 0x55;
DEV float vmax(float a, float b) { float r; asm("v_max_f32 %0, %1, %2" : "=v"(r) : "v"(a), "v"(b)); return r; }
DEV float vmax3(float a, float b, float c) { float r; asm("v_max3_f32 %0, %1, %2, %3" : "=v"(r) : "v"(a), "v"(b), "v"(c)); return r; }
#define X1632 "v_mov_b32 %1, %0\n\ts_nop 1\n\tv_permlane16_swap_b32 %0, %1\n\tv_max_f32 %0, %0, %1\n\tv_mov_b32 %1, %0\n\ts_nop 1\n\t" \
              "v_permlane32_swap_b32 %0, %1\n\tv_max_f32 %0, %0, %1"
DEV float max4_x1632(float a, float b, float c, float d) {
  float r, t;
  asm("v_max3_f32 %0, %2, %3, %4\n\tv_max_f32 %0, %0, %5\n\t" X1632 : "=&v"(r), "=&v"(t) : "v"(a), "v"(b), "v"(c), "v"(d));
  return r;
}
DEV float max_all_but_bit1(float x) {
  float r, t;
  asm("s_nop 1\n\tv_max_f32_dpp %0, %2, %2 quad_perm:[1,0,3,2] row_mask:0xf bank_mask:0xf\n\ts_nop 1\n\t"
      "v_max_f32_dpp %0, %0, %0 row_ror:4 row_mask:0xf bank_mask:0xf\n\ts_nop 1\n\t"
      "v_max_f32_dpp %0, %0, %0 row_ror:8 row_mask:0xf bank_mask:0xf\n\t" X1632 : "=&v"(r), "=&v"(t) : "v"(x));
  return r;
}
DEV float swap16_add(float a, float b) { auto s = __builtin_amdgcn_permlane16_swap(asu(a), asu(b), false, false); return asf(s[0]) + asf(s[1]); }
DEV float swap32_add(float a, float b) { auto s = __builtin_amdgcn_permlane32_swap(asu(a), asu(b), false, false); return asf(s[0]) + asf(s[1]); }
DEV float x16_sum(float x) { return swap16_add(x, x); }
DEV float x32_sum(float x) { return swap32_add(x, x); }
DEV float oct_sum(float x) { x += dppf<XOR1>(x); x += dppf<XOR2>(x); x += dppf<HMIR>(x); return x; }
DEV float row_sum(float x) { x = oct_sum(x); x += dppf<ROR8>(x); return x; }
DEV float wave_sum(float x) { return x32_sum(x16_sum(row_sum(x))); }
DEV f4 mfma(v4u a, v4u b, f4 c) { return __builtin_amdgcn_mfma_f32_16x16x32_bf16(__builtin_bit_cast(bf8, a), __builtin_bit_cast(bf8, b), c, 0, 0, 0); }
#define SB() __builtin_amdgcn_sched_barrier(0)
#define BAR() asm volatile("s_waitcnt lgkmcnt(0)\n\ts_barrier" ::: "memory")
#define BAR0() asm volatile("s_barrier" ::: "memory")
#define CFENCE() asm volatile("" ::: "memory")
#define TOUCH(x) asm volatile("" : "+v"(x))
template <int N> DEV void vmwait() { asm volatile("s_waitcnt vmcnt(%0)" :: "n"(N) : "memory"); }
#define TOUCHS(x) asm volatile("" : "+s"(x))

typedef __amdgpu_buffer_rsrc_t rsrc_t;
DEV rsrc_t mkrs(const void* p, unsigned bytes) { return __builtin_amdgcn_make_buffer_rsrc((void*)p, 0, bytes, 0x00020000); }
DEV v4u ldw(rsrc_t r, int voff, int soff) { return __builtin_amdgcn_raw_buffer_load_b128(r, voff, soff, W_AUX); }
DEV v4u ldx(rsrc_t r, int voff, int soff = 0) { return __builtin_amdgcn_raw_buffer_load_b128(r, voff, soff, AUX_SC1); }
DEV void stx(rsrc_t r, int voff, v4u v) { __builtin_amdgcn_raw_buffer_store_b128(v, r, voff, 0, AUX_SC1); }
struct Tag { unsigned t, m; };
DEV unsigned tag1(float f, const Tag& g) { return __builtin_amdgcn_bitop3_b32(g.m, g.t, asu(f), 0xca); }
DEV v4u tag4(f4 v, const Tag& g) { return v4u{tag1(v[0], g), tag1(v[1], g), tag1(v[2], g), tag1(v[3], g)}; }
DEV unsigned xo1(unsigned v, unsigned t, unsigned acc) { return __builtin_amdgcn_bitop3_b32(v, t, acc, 0xbe); }
DEV unsigned chk4(v4u v, const Tag& g) { return xo1(v[3], g.t, xo1(v[2], g.t, xo1(v[1], g.t, v[0] ^ g.t))); }
DEV unsigned chk4(v4u v, const Tag& g, unsigned acc) { return xo1(v[3], g.t, xo1(v[2], g.t, xo1(v[1], g.t, xo1(v[0], g.t, acc)))); }
DEV bool isbad(unsigned acc) { return (acc & TAGM) != 0; }
DEV f4 val4(v4u v) { return f4{asf(v[0] & ~TAGM), asf(v[1] & ~TAGM), asf(v[2] & ~TAGM), asf(v[3] & ~TAGM)}; }

template <int I> struct IC { static constexpr int v = I; };
template <int LO, int HI, class F> DEV void sfor(F&& f) { if constexpr (LO < HI) { f(IC<LO>{}); sfor<LO + 1, HI>(f); } }
constexpr int imin(int a, int b) { return a < b ? a : b; }
constexpr int imax(int a, int b) { return a > b ? a : b; }
constexpr int lateA[8] = LATE_A, lateB[8] = LATE_B;
constexpr int upA(int t) { return t < 0 ? WAL : t >= X1_AT ? 17 : imin(16, imax(upA(t - 1), t < 7 ? imin(8 + WAK, t + 1 + WA1) : t + 1 + WAK)); }
constexpr int upB(int t) { return t < 0 ? imax(WB0, WBE) : imin(16 + WB2, imax(upB(t - 1), t + 1 + (t < WBSW ? WBE : WB1))); }
constexpr int lpos(int t, int half) { return t + 4 * (1 - half); }
constexpr int l_t(int k) { return k & 3; }
constexpr int l_half(int k) { return 1 - (k >> 2); }
constexpr int g_rg(int k) { return k >> 2; }
constexpr int g_tile(int k) { return (k & 3) == 0 ? 1 : (k & 3) == 1 ? 0 : k & 3; }

struct Masks { unsigned m[4]; };
DEV v4u aop(unsigned xr, const Masks& k) { return v4u{xr & k.m[0], xr & k.m[1], xr & k.m[2], xr & k.m[3]}; }

DEV unsigned voff_b(unsigned row, unsigned ch) { return 256u * row + 16u * (ch ^ (((row & 3) << 2) | ((row >> 2) & 3))); }
template <class V> DEV V lds_ld(unsigned a) { return *(LAS V*)(size_t)a; }
template <class V> DEV void lds_st(unsigned a, V v) { *(LAS V*)(size_t)a = v; }

DEV unsigned layernorm(unsigned xw, unsigned own, unsigned lw, unsigned lr) {
  const float a = bflo(xw), b = bfhi(xw);
  const float mw = wave_sum(a + b) * (1.f / 128);
  const float da = a - mw, db = b - mw;
  lds_st<f2>(lw, f2{mw, wave_sum(fmaf(da, da, db * db))});
  BAR();
  const f2 p = lds_ld<f2>(lr);
  const float mean = oct_sum(p[0]) * 0.125f;
  const float d = p[0] - mean;
  const float var = oct_sum(fmaf(d * d, 128.f, p[1])) * (1.f / D);
  const float den = rbf(__builtin_amdgcn_sqrtf(rbf(rbf(var) + 1e-5f)));
  const float xn = rbf(asf(own << 16) - rbf(mean)) * __builtin_amdgcn_rcpf(den);
  return pk2(xn, xn);
}

__global__ void __launch_bounds__(NTHR, 2)
fused_k(const u16* x_, const u16* wq_, const u16* wg_, const u16* wu_, unsigned char* ws_, const u16* kc_, const u16* vc_, const u16* wo_,
        const u16* wd_, u16* out_) {
  __shared__ __attribute__((aligned(16))) unsigned char lds_raw[LDS_BYTES];
  const unsigned L = (unsigned)(size_t)(LAS unsigned char*)lds_raw;
  const u64x8 ka = *(const volatile CAS u64x8*)__builtin_amdgcn_kernarg_segment_ptr();
  const u64x2 kb = ((const volatile CAS u64x2*)__builtin_amdgcn_kernarg_segment_ptr())[4];
  SB();
  const int tid = threadIdx.x, lane = tid & 63, w = __builtin_amdgcn_readfirstlane(tid >> 6);
  const int gp = lane >> 4, c = lane & 15;
  const int b = blockIdx.x, xcd = b & 7, li = b >> 3;
  int mb = -(li >> 4); TOUCHS(mb);
  auto pick = [&](int a, int bb) INL { return a ^ ((a ^ bb) & mb); };
  const int g = xcd >> 1, j = (xcd & 1) * 16 + li, ct = j & 7, ks = j >> 3;
  const int idx = xcd * 14 + (li - 16), SG = idx >> 4, r = idx & 15;
  const int row = pick(256 * ks + 32 * w, 128 * (r & 7) + 16 * w) + gp;
  int ownoff = (row + 4 * (c & pick(7, 3))) * 2; TOUCH(ownoff);
  int vo0 = __umul24(row, pick(8192, 7168)) + pick((8 * g + ct) * 256, SG * 1024) + 16 * c; TOUCH(vo0);
  int xoff = tid * 4; TOUCH(xoff);
  int so0[NCOM];
#pragma unroll
  for (int k = 0; k < NCOM; ++k) { so0[k] = pick(k * 4 * 8192, g_tile(k) * 256); TOUCHS(so0[k]); }
  unsigned n0 = pick(D * HQ * DH * 2, D * DF * 2); TOUCHS(n0);
  int up = r >> 3; TOUCHS(up);
  SB();
  const u16* x = (const u16*)ka[0];
  unsigned char* ws = (unsigned char*)ka[4];
  const rsrc_t rX = mkrs(x, D * 2);
  unsigned xw = __builtin_amdgcn_raw_buffer_load_b32(rX, xoff, 0, 0);
  const unsigned own = __builtin_amdgcn_raw_buffer_load_b16(rX, ownoff, 0, 0);
  SB();
  const u64 pb = up ? ka[3] : ka[2];
  const rsrc_t r0 = mkrs((const void*)(mb ? pb : ka[1]), n0);
  SB();
  v4u w0[NCOM];
  sfor<0, NCOM>([&](auto I) { SB(); BAR0(); w0[decltype(I)::v] = ldw(r0, vo0, so0[decltype(I)::v]); });
  SB();
  const rsrc_t rW = mkrs(ws, WS_END);
  unsigned e0;
  Tag tg;
  Masks mk;
  unsigned xl;
  v4u wl[8];
  unsigned l3w, l3r;
  int col;
  unsigned xo;
  GAS u64* accp;
  u16* const out = (u16*)kb[1];
  v4u flag;
  int flg;
  auto load_e = [&]() INL { e0 = __builtin_amdgcn_raw_buffer_load_b32(rW, WS_E, 0, 0); };
  auto ln = [&]() INL {
    unsigned lw = L + L_LN + tid * 8, lr = L + L_LN + (lane & 7) * 512; TOUCH(lw); TOUCH(lr);
    SB();
    TOUCH(xw);
    return layernorm(xw, own, lw, lr);
  };
  auto setup0 = [&]() INL {
    int cc = c; TOUCH(cc);
#pragma unroll
    for (int d = 0; d < 4; ++d) mk.m[d] = (cc == 2 * d) ? 0x0000ffffu : (cc == 2 * d + 1) ? 0xffff0000u : 0u;
  };
  auto setup1 = [&](int cq) INL {
    tg.t = (__builtin_amdgcn_readfirstlane(e0) + 1u) & TAGM;
    tg.m = TAGM; TOUCH(tg.m);
    l3w = L + L_RED3 + w * 1024 + (gp >> 1) * 512 + (8 * c + 4 * (gp & 1)) * 4; TOUCH(l3w);
    l3r = L + L_RED3 + (tid & 255) * 4; TOUCH(l3r);
    col = cq * 256 + (tid & 255); TOUCH(col);
    accp = (GAS u64*)(ws + WS_ACC + (col >> 3) * 128 + (col & 7) * 8); TOUCH(accp);
    flg = WS_FLAG; TOUCH(flg);
  };
  auto check_flag = [&]() INL {
    if (__builtin_expect(__any(isbad(chk4(flag, tg))), 0)) {
      int spins = 0;
      do { CFENCE(); __builtin_amdgcn_s_sleep(POLL_SLEEP); flag = ldx(rW, flg); } while (__any(isbad(chk4(flag, tg))) & (++spins < SPIN_CAP));
    }
  };

  auto last_gemv = [&]() INL {
    f4 a0 = {0.f, 0.f, 0.f, 0.f}, a1 = {0.f, 0.f, 0.f, 0.f};
    sfor<0, 4>([&](auto I) {
      constexpr int t = decltype(I)::v;
      const v4u a = aop(dppu<NBC + t>(xl), mk);
      a0 = mfma(a, wl[lpos(t, 0)], a0);
      a1 = mfma(a, wl[lpos(t, 1)], a1);
    });
    f4 z;
#pragma unroll
    for (int i = 0; i < 4; ++i) z[i] = asf(__builtin_amdgcn_permlane32_swap(asu(a0[i]), asu(a1[i]), false, false)[0]);
    lds_st<f4>(l3w, z);
    BAR();
    if (w < 4) {
      f2 p[4];
#pragma unroll
      for (int i = 0; i < 4; ++i) p[i] = f2{lds_ld<float>(l3r + i * 2048), lds_ld<float>(l3r + i * 2048 + 1024)};
      SB();
      const f2 s2 = (p[0] + p[1]) + (p[2] + p[3]);
      const float s = s2[0] + s2[1];
      constexpr double MAGIC = 6291456.0;
      constexpr long long MBITS = 0x4158000000000000ll;
      const double sd = (double)__builtin_amdgcn_fmed3f(s, -1048576.f, 1048576.f) + MAGIC;
      const u64 v = ((u64)(__builtin_bit_cast(long long, sd) - MBITS) << 6) | 1ull;
      const u64 old = __hip_atomic_fetch_add(accp, v, __ATOMIC_RELAXED, __HIP_MEMORY_SCOPE_AGENT);
      if ((old & 63) == 59) {
        const long long tot = (long long)(old + v) >> 6;
        const float y = (float)(__builtin_bit_cast(double, tot + MBITS) - MAGIC) + asf(xo << 16);
        __builtin_amdgcn_raw_buffer_store_b16((u16)pk2(y, y), mkrs(out, D * 2), col * 2, 0, 0);
        if (col == 0) __builtin_amdgcn_raw_buffer_store_b32(tg.t, rW, WS_E, 0, AUX_SC1);
      }
    }
  };

  if (__builtin_expect(li < 16, 1)) {
    v4u wqv[8], kv[4], vv[4], q0, q1;
    sfor<0, NCOM>([&](auto I) { wqv[decltype(I)::v] = w0[decltype(I)::v]; });
    rsrc_t rk, rv, ro;
    int vokv, voo, x1off;
    auto issue = [&](auto I) {
      constexpr int k = decltype(I)::v;
      if constexpr (k < 8) wqv[k] = ldw(r0, vo0, k * 4 * 8192);
      else if constexpr (k < 12) kv[k - 8] = ldw(rk, vokv, (k - 8) * 1024);
      else if constexpr (k < 16) vv[k - 12] = ldw(rv, vokv, (k - 12) * 1024);
      else if constexpr (k == 16) { q0 = ldx(rW, x1off); q1 = ldx(rW, x1off, px(4096)); flag = ldx(rW, flg); xo = __builtin_amdgcn_raw_buffer_load_b16(rX, col * 2, 0, 0); }
      else wl[k - 17] = ldw(ro, voo, l_t(k - 17) * 8192 + l_half(k - 17) * 256);
    };
    sfor<NCOM, WA0>([&](auto I) { SB(); BAR0(); issue(I); });
    SB();
    load_e();
    SB();
    const unsigned xn = ln();
    SB();
    sfor<WA0, upA(-1)>(issue);
    SB();
    setup0();
    SB();
    const int cq = j & 3, slot = 8 * g + (j >> 2), kg = 2 * (gp & 1) + (gp >> 1);
    unsigned lrw, lrr;
    int x1w;
    f4 acc = {0.f, 0.f, 0.f, 0.f};
    sfor<0, 8>([&](auto I) {
      constexpr int t = decltype(I)::v;
      acc = mfma(aop(dppu<NBC + t>(xn), mk), wqv[t], acc);
      SB();
      if constexpr (t == 0) {
        rk = mkrs((const void*)ka[5], HKV * SK * DH * 2); rv = mkrs((const void*)ka[6], HKV * SK * DH * 2);
        vokv = ((g * SK + (SK - WIN) + 128 * j + 16 * w) * DH) * 2 + lane * 16;
      }
      sfor<upA(t - 1), upA(t)>(issue);
      if constexpr (t == 4) {
        setup1(cq);
        lrw = L + L_RED + (lane < 32 ? w * 512 + (8 * c + 4 * gp) * 4 : 4096 + c * 16); TOUCH(lrw);
        lrr = L + L_RED + (lane >> 5) * 2048 + (lane & 31) * 16; TOUCH(lrr);
        x1w = lane < 32 ? WS_X1 + px(((g * 4 + ks) * 1024 + ct * 128) * 4 + lane * 16) : WS_END; TOUCH(x1w);
      }
      SB();
    });
    lds_st<f4>(lrw, acc);
    BAR();
    if (w == 0) {
      const f4 p0 = lds_ld<f4>(lrr), p1 = lds_ld<f4>(lrr + 512), p2 = lds_ld<f4>(lrr + 1024), p3 = lds_ld<f4>(lrr + 1536);
      f4 s = (p0 + p1) + (p2 + p3);
      s[0] = x32_sum(s[0]); s[1] = x32_sum(s[1]); s[2] = x32_sum(s[2]); s[3] = x32_sum(s[3]);
      stx(rW, x1w, tag4(s, tg));
    }
    SB();
    ro = mkrs((const void*)ka[7], HQ * DH * D * 2);
    voo = (128 * slot + 16 * w + kg) * 2048 + (256 * cq + 8 * c) * 2; TOUCH(voo);
    x1off = WS_X1 + px(((g * 4 + 2 * (lane >> 5)) * 1024 + w * 128) * 4 + (lane & 31) * 16);
    unsigned lqw = L + L_Q + w * 272 + (lane & 31) * 8 + (lane >> 5) * 4; TOUCH(lqw);
    unsigned lqr = L + L_Q + (c & 7) * 272 + gp * 16; TOUCH(lqr);
    unsigned lmw = L + L_MX + ((c & 7) * 8 + w) * 4; TOUCH(lmw);
    unsigned lmr = L + L_MX + (c & 7) * 32; TOUCH(lmr);
    unsigned lpw = L + L_P + (c & 7) * 272 + (16 * w + 4 * gp) * 2; TOUCH(lpw);
    unsigned lv0 = L + L_V + voff_b(8 * gp + (c >> 2), 2 * w + ((c & 3) >> 1)) + 8 * (c & 1); TOUCH(lv0);
    unsigned lv1 = L + L_V + voff_b(8 * gp + (c >> 2) + 4, 2 * w + ((c & 3) >> 1)) + 8 * (c & 1); TOUCH(lv1);
    int x2w = c < 8 ? WS_X2O + px((((((g * 4 + (c >> 1)) * 8 + w) * 32 + j) * 2 + (c & 1)) * 16 + 4 * gp) * 4)
                    : (w == 0 && gp == 0) ? WS_X2M + px(((g * 8 + c - 8) * 32 + j) * 16) : WS_END; TOUCH(x2w);
    v4u ones = {0x3f803f80u, 0x3f803f80u, 0x3f803f80u, 0x3f803f80u}; TOUCH(ones);
    int x2o = WS_X2O + px(((slot >> 1) * 8 + w) * 4096 + (lane >> 2) * 128 + (slot & 1) * 64 + (lane & 3) * 16); TOUCH(x2o);
    int x2m = WS_X2M + px((slot * 32 + (lane >> 2) + 16 * (lane & 1)) * 16); TOUCH(x2m);
    SB();
    sfor<8, 16>([&](auto I) {
      constexpr int t = decltype(I)::v;
      lds_st<v4u>(L + (t < 12 ? L_K : L_V) + voff_b(16 * w + 4 * (t & 3) + gp, c), t < 12 ? kv[t & 3] : vv[t & 3]);
      SB();
      sfor<upA(t - 1), upA(t)>(issue);
      if constexpr (t == 11) {
#pragma unroll
        for (int u = 0; u < 4; ++u) kv[u] = lds_ld<v4u>(L + L_K + voff_b(16 * w + c, 4 * u + gp));
      }
      SB();
    });
    {
      int spins = 0;
      bool bad = isbad(chk4(q1, tg, chk4(q0, tg)));
      if (__builtin_expect(__any(bad), 0)) {
        do {
          CFENCE();
          if (bad) { q0 = ldx(rW, x1off); q1 = ldx(rW, x1off, px(4096)); bad = isbad(chk4(q1, tg, chk4(q0, tg))); }
        } while (__any(bad) & (++spins < SPIN_CAP));
      }
    }
    {
      const f4 s = val4(q0) + val4(q1);
      lds_st<unsigned>(lqw, pk2(swap32_add(s[0], s[2]), swap32_add(s[1], s[3])));
    }
    SB();
    sfor<17, 17 + WA2>(issue);
    SB();
    BAR();
    f4 sc;
    v4u va[4];
    {
      const v4u qa = lds_ld<v4u>(lqr), qb = lds_ld<v4u>(lqr + 64), qc = lds_ld<v4u>(lqr + 128), qd = lds_ld<v4u>(lqr + 192);
#pragma unroll
      for (int t = 0; t < 4; ++t) {
        const v2u u0 = __builtin_bit_cast(v2u, __builtin_amdgcn_ds_read_tr16_b64_v4i16((LAS s4*)(size_t)(lv0 + t * 8192)));
        const v2u u1 = __builtin_bit_cast(v2u, __builtin_amdgcn_ds_read_tr16_b64_v4i16((LAS s4*)(size_t)(lv1 + t * 8192)));
        va[t] = v4u{u0[0], u0[1], u1[0], u1[1]};
      }
      SB();
      sc = mfma(kv[0], qa, f4{0.f, 0.f, 0.f, 0.f});
      sc = mfma(kv[1], qb, sc);
      sc = mfma(kv[2], qc, sc);
      sc = mfma(kv[3], qd, sc);
      const unsigned a = pk2(sc[0], sc[1]), bq = pk2(sc[2], sc[3]);
      const f2 sa = f2{bflo(a), bfhi(a)} * 0.08838834764831845f, sb = f2{bflo(bq), bfhi(bq)} * 0.08838834764831845f;
      const unsigned a2 = pk2(sa[0], sa[1]), b2 = pk2(sb[0], sb[1]);
      sc = f4{bflo(a2), bfhi(a2), bflo(b2), bfhi(b2)};
    }
    lds_st<float>(lmw, max4_x1632(sc[0], sc[1], sc[2], sc[3]));
    BAR();
    float m;
    {
      const f4 a = lds_ld<f4>(lmr), bq = lds_ld<f4>(lmr + 16);
      m = vmax3(vmax3(a[0], a[1], a[2]), vmax3(bq[0], bq[1], bq[2]), vmax(a[3], bq[3]));
      const f4 e = (sc - m) * LOG2E;
      lds_st<v2u>(lpw, v2u{pk2(__builtin_amdgcn_exp2f(e[0]), __builtin_amdgcn_exp2f(e[1])), pk2(__builtin_amdgcn_exp2f(e[2]), __builtin_amdgcn_exp2f(e[3]))});
    }
    SB();
    sfor<17 + WA2, 17 + WA2M>(issue);
    SB();
    BAR();
    {
      v4u pa[4];
#pragma unroll
      for (int t = 0; t < 4; ++t) pa[t] = lds_ld<v4u>(lqr + (L_P - L_Q) + t * 64);
      SB();
      f4 ov = {0.f, 0.f, 0.f, 0.f}, ls = {0.f, 0.f, 0.f, 0.f};
#pragma unroll
      for (int t = 0; t < 4; ++t) { ov = mfma(va[t], pa[t], ov); ls = mfma(ones, pa[t], ls); }
      if (c >= 8) { ov[0] = m; ov[1] = ls[0]; }
      stx(rW, x2w, tag4(ov, tg));
    }
    check_flag();
    {
      SB();
      auto paced = [&](auto I) {
        constexpr int k = decltype(I)::v;
        if constexpr (lateA[k] >= 0) { vmwait<k - 1 - lateA[k] + (lateA[k] < WA2M)>(); }
        SB(); issue(IC<17 + k>{}); SB();
      };
      sfor<WA2M, 8>(paced);
      TOUCH(wl[7]);
      SB();
      v4u ml, o0, o1;
      auto look = [&]() INL {
        ml = ldx(rW, x2m); o0 = ldx(rW, x2o); o1 = ldx(rW, x2o, px(2048));
        return isbad(chk4(o1, tg, chk4(o0, tg, chk4(ml, tg))));
      };
      bool bad = look();
      int spins = 0;
      if (__builtin_expect(__any(bad), 0)) {
        do {
          CFENCE();
          __builtin_amdgcn_s_sleep(POLL_SLEEP);
          if (bad) bad = look();
        } while (__any(bad) & (++spins < SPIN_CAP));
      }
      const float ms = asf(ml[0] & ~TAGM), ls = asf(ml[1] & ~TAGM);
      const float M = max_all_but_bit1(ms);
      const float e = __builtin_amdgcn_exp2f((ms - M) * LOG2E);
      float Lt = e * ls;
      Lt += dppf<XOR1>(Lt); Lt += dppf<ROR4>(Lt); Lt += dppf<ROR8>(Lt);
      Lt = x32_sum(x16_sum(Lt));
      const f4 z = val4(o0) * dppf<QB0>(e) + val4(o1) * dppf<QB1>(e);
      float o = swap16_add(swap32_add(z[0], z[1]), swap32_add(z[2], z[3]));
      o += dppf<ROR4>(o); o += dppf<ROR8>(o);
      o *= __builtin_amdgcn_rcpf(Lt);
      xl = pk2(o, o);
    }
    last_gemv();
    return;
  }
  if (li >= 30) {
    load_e();
    stx(rW, WS_ACC + tid * 16, v4u{0u, 0u, 0u, 0u});
    stx(rW, WS_ACC + 8192 + tid * 16, v4u{0u, 0u, 0u, 0u});
    asm volatile("s_waitcnt vmcnt(0)\n\ts_barrier" :: "v"(w0[0]), "v"(w0[NCOM - 1]), "v"(w0[NCOM / 2]), "v"(own), "v"(xw) : "memory");
    const unsigned t = (e0 + 1u) & TAGM;
    if (tid < 8) stx(rW, WS_FLAG + tid * 16, v4u{t, t, t, t});
    __builtin_amdgcn_s_waitcnt(0x0F70);
    return;
  }
  {
    v4u gu[16];
    sfor<0, NCOM>([&](auto I) { gu[decltype(I)::v] = w0[decltype(I)::v]; });
    rsrc_t rd;
    int vod;
    auto issue = [&](auto I) {
      constexpr int k = decltype(I)::v;
      if constexpr (k < 16) gu[k] = ldw(r0, vo0, g_rg(k) * 4 * 7168 + g_tile(k) * 256);
      else wl[k - 16] = ldw(rd, vod, l_t(k - 16) * 8192 + l_half(k - 16) * 256);
    };
    static_assert(NCOM <= WB0 && NCOM <= WA0, "common loads");
    sfor<NCOM, WB0>([&](auto I) { SB(); BAR0(); issue(I); });
    SB();
    load_e();
    SB();
    const int G = 4 * SG + (r >> 2), cq = r & 3, kg = 2 * (gp & 1) + (gp >> 1);
    rd = mkrs((const void*)kb[0], DF * D * 2);
    vod = (128 * G + 16 * w + kg) * 2048 + (256 * cq + 8 * c) * 2; TOUCH(vod);
    const unsigned xn = ln();
    setup0();
    unsigned lgw = L + w * 2048 + (gp >> 1) * 512 + (8 * c + 4 * (gp & 1)) * 4; TOUCH(lgw);
    unsigned lgr = L + tid * 16; TOUCH(lgr);
    int x3w = WS_X3 + px(idx * 2048 + tid * 16); TOUCH(x3w);
    int x3r = WS_X3 + px((SG * 16 + (lane >> 2)) * 2048 + (32 * (r >> 2) + 4 * w + (lane & 3)) * 16); TOUCH(x3r);
    SB();
    sfor<WB0, upB(-1)>([&](auto I) { SB(); BAR0(); issue(I); });
    SB();
    f4 acc[4] = {{0.f, 0.f, 0.f, 0.f}, {0.f, 0.f, 0.f, 0.f}, {0.f, 0.f, 0.f, 0.f}, {0.f, 0.f, 0.f, 0.f}};
    v4u ax;
    sfor<0, 16>([&](auto I) {
      constexpr int t = decltype(I)::v;
      if constexpr (t == 0 || g_rg(t) != g_rg(t - 1)) ax = aop(dppu<NBC + g_rg(t)>(xn), mk);
      acc[g_tile(t)] = mfma(ax, gu[t], acc[g_tile(t)]);
      SB();
      sfor<upB(t - 1), upB(t)>(issue);
      if constexpr (t == 1) setup1(cq);
      SB();
    });
    flag = ldx(rW, flg);
    xo = __builtin_amdgcn_raw_buffer_load_b16(rX, col * 2, 0, 0);
    sfor<upB(15), 16 + WB2>(issue);
    SB();
    {
      f4 z0, z1;
#pragma unroll
      for (int i = 0; i < 4; ++i) {
        z0[i] = asf(__builtin_amdgcn_permlane32_swap(asu(acc[0][i]), asu(acc[1][i]), false, false)[0]);
        z1[i] = asf(__builtin_amdgcn_permlane32_swap(asu(acc[2][i]), asu(acc[3][i]), false, false)[0]);
      }
      lds_st<f4>(lgw, z0); lds_st<f4>(lgw + 1024, z1);
    }
    BAR();
    if (w < 2) {
      f4 p[8];
#pragma unroll
      for (int i = 0; i < 8; ++i) p[i] = lds_ld<f4>(lgr + i * 2048);
      SB();
      const f4 s = ((p[0] + p[1]) + (p[2] + p[3])) + ((p[4] + p[5]) + (p[6] + p[7]));
      stx(rW, x3w, tag4(s, tg));
    }
    SB();
    auto paced = [&](auto I) {
      constexpr int k = decltype(I)::v;
      if constexpr (lateB[k] >= 0) { vmwait<k - 1 - lateB[k] + 2 * (lateB[k] < WB2)>(); }
      SB(); issue(IC<16 + k>{}); SB();
    };
    sfor<WB2, 8>(paced);
    check_flag();
    TOUCH(wl[7]);
    SB();
    v4u hv = ldx(rW, x3r);
    bool bad = isbad(chk4(hv, tg));
    int spins = 0;
    if (__builtin_expect(__any(bad), 0)) {
      do {
        CFENCE();
        __builtin_amdgcn_s_sleep(POLL_SLEEP);
        if (bad) { hv = ldx(rW, x3r); bad = isbad(chk4(hv, tg)); }
      } while (__any(bad) & (++spins < SPIN_CAP));
    }
    {
      const f4 z = val4(hv);
      auto sw = __builtin_amdgcn_permlane32_swap(asu(swap16_add(z[0], z[2])), asu(swap16_add(z[1], z[3])), false, false);
      float gg = asf(sw[0]), uu = asf(sw[1]);
      gg += dppf<ROR4>(gg); uu += dppf<ROR4>(uu);
      gg += dppf<ROR8>(gg); uu += dppf<ROR8>(uu);
      const unsigned gub = pk2(gg, uu);
      gg = bflo(gub); uu = bfhi(gub);
      const float y = gg * fmaf(gg * gg, 0.044715f * 0.7978845608028654f * -2.f * LOG2E, 0.7978845608028654f * -2.f * LOG2E);
      const float ge = gg * __builtin_amdgcn_rcpf(1.f + __builtin_amdgcn_exp2f(y));
      const float hh = rbf(ge) * uu;
      xl = pk2(hh, hh);
    }
    last_gemv();
  }
}

#define LAUNCH_ARGS(d_in, d_out, d_ws) (const u16*)d_in[0], (const u16*)d_in[3], (const u16*)d_in[6], (const u16*)d_in[7], (unsigned char*)d_ws, \
                                       (const u16*)d_in[1], (const u16*)d_in[2], (const u16*)d_in[4], (const u16*)d_in[8], (u16*)d_out
extern "C" void kernel_launch(void* const* d_in, const int* in_sizes, int n_in, void* d_out, int out_size, void* d_ws, size_t ws_size,
                              hipStream_t stream) {
  fused_k<<<NWG, NTHR, 0, stream>>>(LAUNCH_ARGS(d_in, d_out, d_ws));
}
```
